```python
import torch

def _softmax(x, axis=-1):
    return torch.softmax(x, dim=axis)
def _gelu(x):
    return torch.nn.functional.gelu(x, approximate="tanh")
def _silu(x):
    return torch.nn.functional.silu(x)
def _sigmoid(x):
    return torch.sigmoid(x)
def _layernorm(x, axis=-1, eps=1e-5):
    m = x.mean(dim=axis, keepdim=True)
    v = x.var(dim=axis, keepdim=True, unbiased=False)
    return (x - m) / torch.sqrt(v + eps)
def _scatter_add(tgt, idx, src, axis=0):
    return tgt.clone().index_add_(axis, idx, src)
def _segment_sum(src, seg, n):
    out = torch.zeros((n,) + src.shape[1:], dtype=src.dtype)
    return out.index_add_(0, seg, src)
def _pad(x, pw):
    flat = tuple(v for pair in reversed(pw) for v in pair)
    return torch.nn.functional.pad(x, flat)

kernel_name = 'recurrent_scan__gqa_block_cached__dense_activation_gelu__decode'

def setup_inputs():

    _w = (lambda a: (lambda t: t.to(torch.bfloat16) if t.is_floating_point() else t)(torch.as_tensor(a)))
    def _A_setup():
        B, Sq, D = 8, 1, 512
        Hq, Hkv, Sk, Dh = 32, 8, 4096, 128
        assert Hq % Hkv == 0, (Hq, Hkv)
        return {
            "x": _w((lambda *s: torch.randn(*s, dtype=torch.bfloat16) * 0.05)(B, Sq, D)),
            "k_cache": _w((lambda *s: torch.randn(*s, dtype=torch.bfloat16) * 3.2)(B, Hkv, Sk, Dh)),
            "v_cache": _w((lambda *s: torch.randn(*s, dtype=torch.bfloat16) * 3.2)(B, Hkv, Sk, Dh)),
            "wq": _w((lambda *s: torch.randn(*s, dtype=torch.bfloat16) * 3.2)(D, Hq * Dh) * (D ** -0.5)),
            "wo": _w((lambda *s: torch.randn(*s, dtype=torch.bfloat16) * 3.2)(Hq * Dh, D) * ((Hq * Dh) ** -0.5)),
        }
    _A_in = _A_setup()

    def _B_setup():
        B, S, D, F = 8, 1, 512, 1792
        return {
            "x": _w((lambda *s: torch.randn(*s, dtype=torch.bfloat16) * 0.05)(B, S, D)),
            "w1": _w((lambda *s: torch.randn(*s, dtype=torch.bfloat16) * 3.2)(D, F) * (D ** -0.5)),
            "b1": _w((lambda *s: torch.randn(*s, dtype=torch.bfloat16) * 3.2)(F) * 0.01),
            "w2": _w((lambda *s: torch.randn(*s, dtype=torch.bfloat16) * 0.4)(F, D) * (F ** -0.5)),
            "b2": _w((lambda *s: torch.randn(*s, dtype=torch.bfloat16) * 0.4)(D) * 0.01),
        }
    _B_in = _B_setup()

    return {
        "A_x": _A_in["x"],
        "A_k_cache": _A_in["k_cache"],
        "A_v_cache": _A_in["v_cache"],
        "A_wq": _A_in["wq"],
        "A_wo": _A_in["wo"],
        "B_x": _B_in["x"],
        "B_w1": _B_in["w1"],
        "B_b1": _B_in["b1"],
        "B_w2": _B_in["w2"],
        "B_b2": _B_in["b2"],
    }

def reference(A_x, A_k_cache, A_v_cache, A_wq, A_wo, B_x, B_w1, B_b1, B_w2, B_b2):
    def _A_ref(x, k_cache, v_cache, wq, wo):
        B, Sq, D = x.shape
        Hq, Dh = 32, 128
        Hkv = k_cache.shape[1]
        rep = Hq // Hkv
        q = (lambda x, *d: x.permute(*d))((x @ wq).reshape(B, Sq, Hq, Dh), 0, 2, 1, 3)
        k = (lambda x, n, axis: torch.repeat_interleave(x, n, dim=axis))(k_cache, rep, axis=1)
        v = (lambda x, n, axis: torch.repeat_interleave(x, n, dim=axis))(v_cache, rep, axis=1)
        scores = torch.einsum("bhqd,bhkd->bhqk", q, k) * (Dh ** -0.5)
        p = _softmax(scores, axis=-1)
        o = torch.einsum("bhqk,bhkd->bhqd", p, v)
        return (lambda x, *d: x.permute(*d))(o, 0, 2, 1, 3).reshape(B, Sq, Hq * Dh) @ wo

    def _B_ref(x, w1, b1, w2, b2):
        h = torch.einsum("bsd,df->bsf", x, w1) + b1
        h = _gelu(h)
        return torch.einsum("bsf,fd->bsd", h, w2) + b2

    x = A_x
    xn = _layernorm(x, axis=-1)
    s = _A_ref(xn, A_k_cache, A_v_cache, A_wq, A_wo)
    g = _silu(_B_ref(xn, B_w1, B_b1, B_w2, B_b2))
    return x + s * g
```

```cpp
#include <hip/hip_runtime.h>
#include <hip/hip_bf16.h>
#include <stdint.h>

typedef unsigned int u32;
typedef unsigned short u16;
typedef __attribute__((ext_vector_type(4))) unsigned u32x4;
typedef __attribute__((ext_vector_type(4))) float f32x4;
typedef __attribute__((ext_vector_type(2))) float f32x2;
typedef __attribute__((ext_vector_type(2))) __bf16 bf16x2;

constexpr int NB = 8, DM = 512, HQ = 32, HKV = 8, SK = 4096, DH = 128, FF = 1792;
constexpr int NSPLIT = 4;
constexpr int KPS = SK / NSPLIT;

constexpr size_t WS_CNT = 0;
constexpr size_t WS_Q = 4096;
constexpr size_t WS_H = WS_Q + NB * HQ * DH * 2;
constexpr size_t WS_PML = WS_H + 32768;
constexpr size_t WS_PO = WS_PML + NB * HQ * NSPLIT * 2 * 4;
constexpr size_t WS_PART = WS_PO + (size_t)NB * HQ * NSPLIT * DH * 4;

__device__ __forceinline__ float bf2f(u16 v) { return __uint_as_float(((u32)v) << 16); }
__device__ __forceinline__ float bflo(u32 v) { return __uint_as_float(v << 16); }
__device__ __forceinline__ float bfhi(u32 v) { return __uint_as_float(v & 0xffff0000u); }
__device__ __forceinline__ u16 f2bf(float f) { return __builtin_bit_cast(u16, (__bf16)f); }
__device__ __forceinline__ float bfr(float f) { return (float)(__bf16)f; }

__device__ __forceinline__ float dot2(u32 k, u32 q, float acc) {
  return __builtin_amdgcn_fdot2_f32_bf16(__builtin_bit_cast(bf16x2, k), __builtin_bit_cast(bf16x2, q), acc, false);
}
template <int CTRL> __device__ __forceinline__ float dpp(float x) {
  return __builtin_bit_cast(float, __builtin_amdgcn_mov_dpp(__builtin_bit_cast(int, x), CTRL, 0xf, 0xf, true));
}
constexpr int XOR1 = 0xB1, XOR2 = 0x4E, XOR7 = 0x141, XOR8 = 0x128, ROR4 = 0x124;
__device__ __forceinline__ float xrow16_max(float x) {
  auto s = __builtin_amdgcn_permlane16_swap(__float_as_uint(x), __float_as_uint(x), false, false);
  x = fmaxf(__uint_as_float(s[0]), __uint_as_float(s[1]));
  auto t = __builtin_amdgcn_permlane32_swap(__float_as_uint(x), __float_as_uint(x), false, false);
  return fmaxf(__uint_as_float(t[0]), __uint_as_float(t[1]));
}
__device__ __forceinline__ float xrow16_sum(float x) {
  auto s = __builtin_amdgcn_permlane16_swap(__float_as_uint(x), __float_as_uint(x), false, false);
  x = __uint_as_float(s[0]) + __uint_as_float(s[1]);
  auto t = __builtin_amdgcn_permlane32_swap(__float_as_uint(x), __float_as_uint(x), false, false);
  return __uint_as_float(t[0]) + __uint_as_float(t[1]);
}

template <int N, int OFF> __device__ __forceinline__ void tr_step(float (&v)[64], int lane) {
  const bool up = (lane & OFF) != 0;
#pragma unroll
  for (int i = 0; i < N / 2; ++i) {
    const float keep = up ? v[i + N / 2] : v[i];
    const float send = up ? v[i] : v[i + N / 2];
    v[i] = keep + __shfl_xor(send, OFF);
  }
}

__device__ __forceinline__ void layernorm_to_lds(const u16* __restrict__ x, float* xnT, int tid) {
  const int lane = tid & 63, wave = tid >> 6;
  if (wave < 4) {
#pragma unroll
    for (int rr = 0; rr < 2; ++rr) {
      const int b = wave * 2 + rr;
      const u32x4 v = *reinterpret_cast<const u32x4*>(x + b * DM + lane * 8);
      float f[8];
#pragma unroll
      for (int k = 0; k < 4; ++k) { f[2 * k] = bflo(v[k]); f[2 * k + 1] = bfhi(v[k]); }
      float s = 0.f;
#pragma unroll
      for (int k = 0; k < 8; ++k) s += f[k];
#pragma unroll
      for (int o = 32; o >= 1; o >>= 1) s += __shfl_xor(s, o);
      const float mean = s * (1.f / DM);
      float q = 0.f;
#pragma unroll
      for (int k = 0; k < 8; ++k) { const float d = f[k] - mean; q += d * d; }
#pragma unroll
      for (int o = 32; o >= 1; o >>= 1) q += __shfl_xor(q, o);
      const float var = bfr(q * (1.f / DM));
      const float mb = bfr(mean);
      const float den = bfr(sqrtf(bfr(var + 1e-5f)));
#pragma unroll
      for (int k = 0; k < 8; ++k) xnT[(lane * 8 + k) * 8 + b] = bfr(bfr(f[k] - mb) / den);
    }
  }
}

__global__ void __launch_bounds__(256) k_prep(const u16* __restrict__ x, const u16* __restrict__ wq, const u16* __restrict__ w1,
                                              const u16* __restrict__ b1, unsigned char* __restrict__ ws) {
  __shared__ __attribute__((aligned(16))) float xnT[DM * 8];
  __shared__ __attribute__((aligned(16))) float red[4 * 64 * 3];
  const int tid = threadIdx.x, lane = tid & 63, wave = tid >> 6;
  const int xc = blockIdx.x & 7, j = blockIdx.x >> 3;
  if (blockIdx.x == 0 && tid < 32) reinterpret_cast<u32*>(ws + WS_CNT)[tid * 32] = 0u;
  const int ch = tid & 1, r0 = tid >> 1;
  const int colq = xc * 512 + j * 16 + ch * 8;
  u32x4 wqv[4];
#pragma unroll
  for (int k = 0; k < 4; ++k) wqv[k] = *reinterpret_cast<const u32x4*>(wq + (size_t)(r0 + 128 * k) * (HQ * DH) + colq);
  const bool doh = j < 28;
  const int colh = xc * 224 + j * 8;
  u32x4 w1v[2];
  if (doh) {
#pragma unroll
    for (int k = 0; k < 2; ++k) w1v[k] = *reinterpret_cast<const u32x4*>(w1 + (size_t)(tid + 256 * k) * FF + colh);
  }
  layernorm_to_lds(x, xnT, tid);
  __syncthreads();
  float acc[64];
#pragma unroll
  for (int i = 0; i < 64; ++i) acc[i] = 0.f;
#pragma unroll
  for (int k = 0; k < 4; ++k) {
    const int row = r0 + 128 * k;
    const f32x4 xa = *reinterpret_cast<const f32x4*>(xnT + row * 8), xb = *reinterpret_cast<const f32x4*>(xnT + row * 8 + 4);
    float xv[8] = {xa[0], xa[1], xa[2], xa[3], xb[0], xb[1], xb[2], xb[3]};
#pragma unroll
    for (int e = 0; e < 4; ++e) {
      const float wl = bflo(wqv[k][e]), wh = bfhi(wqv[k][e]);
#pragma unroll
      for (int b = 0; b < 8; ++b) { acc[b * 8 + 2 * e] = fmaf(xv[b], wl, acc[b * 8 + 2 * e]); acc[b * 8 + 2 * e + 1] = fmaf(xv[b], wh, acc[b * 8 + 2 * e + 1]); }
    }
  }
  tr_step<64, 32>(acc, lane); tr_step<32, 16>(acc, lane); tr_step<16, 8>(acc, lane); tr_step<8, 4>(acc, lane); tr_step<4, 2>(acc, lane);
  red[(wave * 64 + lane) * 3 + 0] = acc[0];
  red[(wave * 64 + lane) * 3 + 1] = acc[1];
  if (doh) {
#pragma unroll
    for (int i = 0; i < 64; ++i) acc[i] = 0.f;
#pragma unroll
    for (int k = 0; k < 2; ++k) {
      const int row = tid + 256 * k;
      const f32x4 xa = *reinterpret_cast<const f32x4*>(xnT + row * 8), xb = *reinterpret_cast<const f32x4*>(xnT + row * 8 + 4);
      float xv[8] = {xa[0], xa[1], xa[2], xa[3], xb[0], xb[1], xb[2], xb[3]};
#pragma unroll
      for (int e = 0; e < 4; ++e) {
        const float wl = bflo(w1v[k][e]), wh = bfhi(w1v[k][e]);
#pragma unroll
        for (int b = 0; b < 8; ++b) { acc[b * 8 + 2 * e] = fmaf(xv[b], wl, acc[b * 8 + 2 * e]); acc[b * 8 + 2 * e + 1] = fmaf(xv[b], wh, acc[b * 8 + 2 * e + 1]); }
      }
    }
    tr_step<64, 32>(acc, lane); tr_step<32, 16>(acc, lane); tr_step<16, 8>(acc, lane); tr_step<8, 4>(acc, lane); tr_step<4, 2>(acc, lane); tr_step<2, 1>(acc, lane);
    red[(wave * 64 + lane) * 3 + 2] = acc[0];
  }
  __syncthreads();
  if (tid < 128) {
    const int l = tid >> 1, e = tid & 1;
    const float s = red[(0 * 64 + l) * 3 + e] + red[(1 * 64 + l) * 3 + e] + red[(2 * 64 + l) * 3 + e] + red[(3 * 64 + l) * 3 + e];
    const int v = (l >> 1) * 2 + e, b = v >> 3, col = v & 7;
    reinterpret_cast<u16*>(ws + WS_Q)[b * (HQ * DH) + xc * 512 + j * 16 + (l & 1) * 8 + col] = f2bf(s);
  } else if (tid < 192 && doh) {
    const int l = tid - 128;
    const float s = red[(0 * 64 + l) * 3 + 2] + red[(1 * 64 + l) * 3 + 2] + red[(2 * 64 + l) * 3 + 2] + red[(3 * 64 + l) * 3 + 2];
    const int b = l >> 3, col = l & 7;
    float hp = bfr(bfr(s) + bf2f(b1[colh + col]));
    const float u = 0.7978845608028654f * (hp + 0.044715f * hp * hp * hp);
    const float gl = 0.5f * hp * (1.f + tanhf(u));
    reinterpret_cast<u16*>(ws + WS_H)[b * FF + colh + col] = f2bf(gl);
  }
}

constexpr int AW = 8;
constexpr int TPW = KPS / (16 * AW);
constexpr int MRG_REC = 4 + DH;
__global__ void __launch_bounds__(512) k_attn(const u16* __restrict__ Kc_, const u16* __restrict__ Vc_, unsigned char* __restrict__ ws) {
  __shared__ __attribute__((aligned(16))) float lds[AW * 64 + AW * 4 * MRG_REC];
  const int tid = threadIdx.x, lane = tid & 63, wave = __builtin_amdgcn_readfirstlane(tid >> 6), r = lane >> 4, c = lane & 15;
  const int kvh = blockIdx.x & 7, jj = blockIdx.x >> 3, b = jj >> 2, s = jj & 3;
  constexpr float SCALE = 0.08838834764831845f, LOG2E = 1.4426950408889634f;
  const u16* qg = reinterpret_cast<const u16*>(ws + WS_Q) + b * (HQ * DH) + kvh * 4 * DH + 8 * c;
  u32x4 qpk[4];
#pragma unroll
  for (int g = 0; g < 4; ++g) qpk[g] = *reinterpret_cast<const u32x4*>(qg + g * DH);
  const size_t base = ((size_t)(b * HKV + kvh) * SK + (size_t)s * KPS + wave * 16 + r) * DH + 8 * c;
  const u16* kb = Kc_ + base;
  const u16* vb = Vc_ + base;
  u32x4 Kr[3][4], Vr[3][4];
#define ISSUE(i_) do { _Pragma("unroll") for (int t_ = 0; t_ < 4; ++t_) { \
    Kr[(i_) % 3][t_] = __builtin_nontemporal_load(reinterpret_cast<const u32x4*>(kb + (size_t)((i_) * AW * 16 + t_ * 4) * DH)); \
    Vr[(i_) % 3][t_] = __builtin_nontemporal_load(reinterpret_cast<const u32x4*>(vb + (size_t)((i_) * AW * 16 + t_ * 4) * DH)); } } while (0)
  float m = -INFINITY, l = 0.f, acc[32];
#pragma unroll
  for (int k = 0; k < 32; ++k) acc[k] = 0.f;
  ISSUE(0);
  ISSUE(1);
#pragma unroll
  for (int i = 0; i < TPW; ++i) {
    if (i + 2 < TPW) ISSUE(i + 2);
    float x[16];
#pragma unroll
    for (int t = 0; t < 4; ++t)
#pragma unroll
      for (int g = 0; g < 4; ++g) {
        float d = 0.f;
#pragma unroll
        for (int k = 0; k < 4; ++k) d = dot2(Kr[i % 3][t][k], qpk[g][k], d);
        x[t * 4 + g] = d;
      }
#define TR_STEP(HALF, CTRL, BIT) _Pragma("unroll") for (int h = 0; h < (HALF); ++h) { \
      const float lo_ = x[h], hi_ = x[h + (HALF)]; const float t1_ = lo_ + dpp<CTRL>(lo_), t2_ = hi_ + dpp<CTRL>(hi_); \
      x[h] = (c & (BIT)) ? t2_ : t1_; }
    TR_STEP(8, XOR8, 8) TR_STEP(4, XOR7, 4) TR_STEP(2, XOR2, 2) TR_STEP(1, XOR1, 1)
#undef TR_STEP
    const float sc = bfr(bfr(x[0]) * SCALE) * LOG2E;
    float pm = sc; pm = fmaxf(pm, dpp<ROR4>(pm)); pm = fmaxf(pm, dpp<XOR8>(pm)); pm = xrow16_max(pm);
    const float mn = fmaxf(m, pm);
    const float alpha = __builtin_amdgcn_exp2f(m - mn), p = __builtin_amdgcn_exp2f(sc - mn);
    l = fmaf(l, alpha, p); m = mn;
    {
      const float a0 = dpp<0x00>(alpha), a1 = dpp<0x55>(alpha), a2 = dpp<0xAA>(alpha), a3 = dpp<0xFF>(alpha);
#pragma unroll
      for (int k = 0; k < 8; ++k) { acc[k] *= a0; acc[8 + k] *= a1; acc[16 + k] *= a2; acc[24 + k] *= a3; }
    }
    float* prow = lds + (wave * 4 + r) * 16;
    prow[c] = p;
    asm volatile("s_waitcnt lgkmcnt(0)" ::: "memory");
    f32x4 pr[4];
#pragma unroll
    for (int u = 0; u < 4; ++u) pr[u] = *reinterpret_cast<const f32x4*>(prow + 4 * u);
#pragma unroll
    for (int t = 0; t < 4; ++t) {
      float vf[8];
#pragma unroll
      for (int k = 0; k < 4; ++k) { vf[2 * k] = bflo(Vr[i % 3][t][k]); vf[2 * k + 1] = bfhi(Vr[i % 3][t][k]); }
#pragma unroll
      for (int g = 0; g < 4; ++g) {
#pragma unroll
        for (int k = 0; k < 8; ++k) acc[g * 8 + k] = fmaf(pr[t][g], vf[k], acc[g * 8 + k]);
      }
    }
  }
#undef ISSUE
#pragma unroll
  for (int k = 0; k < 32; ++k) acc[k] = xrow16_sum(acc[k]);
  { float t = l; t += dpp<ROR4>(t); t += dpp<XOR8>(t); l = xrow16_sum(t); }
  float* mrg = lds + AW * 64;
  if (r == 0) {
#pragma unroll
    for (int g = 0; g < 4; ++g) {
      float* rec = mrg + (wave * 4 + g) * MRG_REC;
#pragma unroll
      for (int k = 0; k < 8; k += 4) *reinterpret_cast<f32x4*>(rec + 4 + 8 * c + k) = f32x4{acc[g * 8 + k], acc[g * 8 + k + 1], acc[g * 8 + k + 2], acc[g * 8 + k + 3]};
      if (c == g) { rec[0] = m; rec[1] = l; }
    }
  }
  __syncthreads();
  if (wave < 4) {
    const int g = wave, d0 = 2 * lane;
    float M = -INFINITY;
#pragma unroll
    for (int w = 0; w < AW; ++w) M = fmaxf(M, mrg[(w * 4 + g) * MRG_REC]);
    float L = 0.f, o0 = 0.f, o1 = 0.f;
#pragma unroll
    for (int w = 0; w < AW; ++w) {
      const float* rec = mrg + (w * 4 + g) * MRG_REC;
      const float wt = __builtin_amdgcn_exp2f(rec[0] - M);
      L = fmaf(wt, rec[1], L); o0 = fmaf(wt, rec[4 + d0], o0); o1 = fmaf(wt, rec[4 + d0 + 1], o1);
    }
    const size_t slot = (size_t)(b * HQ + kvh * 4 + g) * NSPLIT + s;
    *reinterpret_cast<f32x2*>(reinterpret_cast<float*>(ws + WS_PO) + slot * DH + d0) = f32x2{o0, o1};
    if (lane == 0) *reinterpret_cast<f32x2*>(reinterpret_cast<float*>(ws + WS_PML) + slot * 2) = f32x2{M, L};
  }
}

__global__ void __launch_bounds__(512) k_tail(const u16* __restrict__ x, const u16* __restrict__ wo, const u16* __restrict__ w2,
                                              const u16* __restrict__ b2, unsigned char* __restrict__ ws, u16* __restrict__ out) {
  __shared__ __attribute__((aligned(16))) float oT[512 * 8];
  __shared__ __attribute__((aligned(16))) float hT[256 * 8];
  __shared__ __attribute__((aligned(16))) float red[8 * 64 * 4];
  __shared__ u32 flag;
  const int tid = threadIdx.x, lane = tid & 63, wave = tid >> 6;
  const int xc = blockIdx.x & 7, cb = blockIdx.x >> 3;
  const int ch = tid & 1, r0 = tid >> 1;
  const int col = cb * 16 + ch * 8;
  u32x4 wov[2], w2v;
#pragma unroll
  for (int k = 0; k < 2; ++k) wov[k] = *reinterpret_cast<const u32x4*>(wo + (size_t)(xc * 512 + r0 + 256 * k) * DM + col);
  const bool do2 = r0 < 224;
  if (do2) w2v = *reinterpret_cast<const u32x4*>(w2 + (size_t)(xc * 224 + r0) * DM + col);
  {
    const int b = wave, g = lane >> 4, d0 = (lane & 15) * 8;
    const size_t slot = (size_t)(b * HQ + xc * 4 + g) * NSPLIT;
    const float* pml = reinterpret_cast<const float*>(ws + WS_PML) + slot * 2;
    const float* po = reinterpret_cast<const float*>(ws + WS_PO) + slot * DH + d0;
    float mm[NSPLIT], ll[NSPLIT]; f32x4 a0[NSPLIT], a1[NSPLIT];
#pragma unroll
    for (int s = 0; s < NSPLIT; ++s) { mm[s] = pml[2 * s]; ll[s] = pml[2 * s + 1]; a0[s] = *reinterpret_cast<const f32x4*>(po + s * DH); a1[s] = *reinterpret_cast<const f32x4*>(po + s * DH + 4); }
    float M = -INFINITY;
#pragma unroll
    for (int s = 0; s < NSPLIT; ++s) M = fmaxf(M, mm[s]);
    float L = 0.f; f32x4 o0 = {0.f, 0.f, 0.f, 0.f}, o1 = {0.f, 0.f, 0.f, 0.f};
#pragma unroll
    for (int s = 0; s < NSPLIT; ++s) { const float w = __builtin_amdgcn_exp2f(mm[s] - M); L = fmaf(w, ll[s], L); o0 += w * a0[s]; o1 += w * a1[s]; }
    const float inv = 1.f / L;
#pragma unroll
    for (int k = 0; k < 4; ++k) { oT[(g * DH + d0 + k) * 8 + b] = bfr(o0[k] * inv); oT[(g * DH + d0 + 4 + k) * 8 + b] = bfr(o1[k] * inv); }
  }
  for (int i = tid; i < 224 * 8; i += 512) { const int b = i / 224, rr = i % 224; hT[rr * 8 + b] = bf2f(reinterpret_cast<const u16*>(ws + WS_H)[b * FF + xc * 224 + rr]); }
  __syncthreads();
  float acc[64];
#pragma unroll
  for (int i = 0; i < 64; ++i) acc[i] = 0.f;
#pragma unroll
  for (int k = 0; k < 2; ++k) {
    const int row = r0 + 256 * k;
    const f32x4 xa = *reinterpret_cast<const f32x4*>(oT + row * 8), xb = *reinterpret_cast<const f32x4*>(oT + row * 8 + 4);
    float xv[8] = {xa[0], xa[1], xa[2], xa[3], xb[0], xb[1], xb[2], xb[3]};
#pragma unroll
    for (int e = 0; e < 4; ++e) {
      const float wl = bflo(wov[k][e]), wh = bfhi(wov[k][e]);
#pragma unroll
      for (int b = 0; b < 8; ++b) { acc[b * 8 + 2 * e] = fmaf(xv[b], wl, acc[b * 8 + 2 * e]); acc[b * 8 + 2 * e + 1] = fmaf(xv[b], wh, acc[b * 8 + 2 * e + 1]); }
    }
  }
  tr_step<64, 32>(acc, lane); tr_step<32, 16>(acc, lane); tr_step<16, 8>(acc, lane); tr_step<8, 4>(acc, lane); tr_step<4, 2>(acc, lane);
  red[(wave * 64 + lane) * 4 + 0] = acc[0];
  red[(wave * 64 + lane) * 4 + 1] = acc[1];
#pragma unroll
  for (int i = 0; i < 64; ++i) acc[i] = 0.f;
  if (do2) {
    const f32x4 xa = *reinterpret_cast<const f32x4*>(hT + r0 * 8), xb = *reinterpret_cast<const f32x4*>(hT + r0 * 8 + 4);
    float xv[8] = {xa[0], xa[1], xa[2], xa[3], xb[0], xb[1], xb[2], xb[3]};
#pragma unroll
    for (int e = 0; e < 4; ++e) {
      const float wl = bflo(w2v[e]), wh = bfhi(w2v[e]);
#pragma unroll
      for (int b = 0; b < 8; ++b) { acc[b * 8 + 2 * e] = xv[b] * wl; acc[b * 8 + 2 * e + 1] = xv[b] * wh; }
    }
  }
  tr_step<64, 32>(acc, lane); tr_step<32, 16>(acc, lane); tr_step<16, 8>(acc, lane); tr_step<8, 4>(acc, lane); tr_step<4, 2>(acc, lane);
  red[(wave * 64 + lane) * 4 + 2] = acc[0];
  red[(wave * 64 + lane) * 4 + 3] = acc[1];
  __syncthreads();
  float* part = reinterpret_cast<float*>(ws + WS_PART);
  if (tid < 256) {
    const int l = tid >> 2, e4 = tid & 3;
    float s = 0.f;
#pragma unroll
    for (int w = 0; w < 8; ++w) s += red[(w * 64 + l) * 4 + e4];
    const int v = (l >> 1) * 2 + (e4 & 1), b = v >> 3, cc = (l & 1) * 8 + (v & 7);
    __hip_atomic_store(part + ((size_t)(cb * 8 + xc) * 2 + (e4 >> 1)) * 128 + b * 16 + cc, s, __ATOMIC_RELAXED, __HIP_MEMORY_SCOPE_AGENT);
    asm volatile("s_waitcnt vmcnt(0)" ::: "memory");
  }
  __syncthreads();
  if (tid == 0) {
    const u32 old = __hip_atomic_fetch_add(reinterpret_cast<u32*>(ws + WS_CNT) + cb * 32, 1u, __ATOMIC_RELAXED, __HIP_MEMORY_SCOPE_AGENT);
    flag = (old == 7u) ? 1u : 0u;
  }
  __syncthreads();
  if (flag == 0u) return;
  if (tid < 128) {
    float ss = 0.f, gg = 0.f;
#pragma unroll
    for (int xx = 0; xx < 8; ++xx) {
      ss += __hip_atomic_load(part + ((size_t)(cb * 8 + xx) * 2 + 0) * 128 + tid, __ATOMIC_RELAXED, __HIP_MEMORY_SCOPE_AGENT);
      gg += __hip_atomic_load(part + ((size_t)(cb * 8 + xx) * 2 + 1) * 128 + tid, __ATOMIC_RELAXED, __HIP_MEMORY_SCOPE_AGENT);
    }
    const int b = tid >> 4, cc = cb * 16 + (tid & 15);
    const float sb = bfr(ss);
    const float f = bfr(bfr(gg) + bf2f(b2[cc]));
    const float g = bfr(f / (1.f + __expf(-f)));
    out[b * DM + cc] = f2bf(bf2f(x[b * DM + cc]) + bfr(sb * g));
  }
}

extern "C" void kernel_launch(void* const* d_in, const int* in_sizes, int n_in,
                              void* d_out, int out_size, void* d_ws, size_t ws_size,
                              hipStream_t stream) {
  const u16* x = (const u16*)d_in[0];
  const u16* kc = (const u16*)d_in[1];
  const u16* vc = (const u16*)d_in[2];
  const u16* wq = (const u16*)d_in[3];
  const u16* wo = (const u16*)d_in[4];
  const u16* w1 = (const u16*)d_in[6];
  const u16* b1 = (const u16*)d_in[7];
  const u16* w2 = (const u16*)d_in[8];
  const u16* b2 = (const u16*)d_in[9];
  unsigned char* ws = (unsigned char*)d_ws;
  k_prep<<<256, 256, 0, stream>>>(x, wq, w1, b1, ws);
  k_attn<<<256, 512, 0, stream>>>(kc, vc, ws);
  k_tail<<<256, 512, 0, stream>>>(x, wo, w2, b2, ws, (u16*)d_out);
}
```

```cpp
#include <hip/hip_runtime.h>
#include <hip/hip_bf16.h>
#include <stdint.h>

typedef unsigned int u32;
typedef unsigned short u16;
typedef __attribute__((ext_vector_type(4))) unsigned u32x4;
typedef __attribute__((ext_vector_type(4))) float f32x4;
typedef __attribute__((ext_vector_type(2))) float f32x2;
typedef __attribute__((ext_vector_type(2))) __bf16 bf16x2;

constexpr int NB = 8, DM = 512, HQ = 32, HKV = 8, SK = 4096, DH = 128, FF = 1792;
constexpr int NSPLIT = 4;
constexpr int KPS = SK / NSPLIT;

constexpr size_t WS_CNT = 0;
constexpr size_t WS_Q = 8192;
constexpr size_t WS_H = WS_Q + NB * HQ * DH * 2;
constexpr size_t WS_PML = WS_H + 32768;
constexpr size_t WS_PO = WS_PML + NB * HQ * NSPLIT * 2 * 4;
constexpr size_t WS_PART = WS_PO + (size_t)NB * HQ * NSPLIT * DH * 4;

__device__ __forceinline__ float bf2f(u16 v) { return __uint_as_float(((u32)v) << 16); }
__device__ __forceinline__ float bflo(u32 v) { return __uint_as_float(v << 16); }
__device__ __forceinline__ float bfhi(u32 v) { return __uint_as_float(v & 0xffff0000u); }
__device__ __forceinline__ u16 f2bf(float f) { return __builtin_bit_cast(u16, (__bf16)f); }
__device__ __forceinline__ float bfr(float f) { return (float)(__bf16)f; }

__device__ __forceinline__ float dot2(u32 k, u32 q, float acc) {
  return __builtin_amdgcn_fdot2_f32_bf16(__builtin_bit_cast(bf16x2, k), __builtin_bit_cast(bf16x2, q), acc, false);
}
template <int CTRL> __device__ __forceinline__ float dpp(float x) {
  return __builtin_bit_cast(float, __builtin_amdgcn_mov_dpp(__builtin_bit_cast(int, x), CTRL, 0xf, 0xf, true));
}
constexpr int XOR1 = 0xB1, XOR2 = 0x4E, XOR7 = 0x141, XOR8 = 0x128, ROR4 = 0x124;
__device__ __forceinline__ float xrow16_max(float x) {
  auto s = __builtin_amdgcn_permlane16_swap(__float_as_uint(x), __float_as_uint(x), false, false);
  x = fmaxf(__uint_as_float(s[0]), __uint_as_float(s[1]));
  auto t = __builtin_amdgcn_permlane32_swap(__float_as_uint(x), __float_as_uint(x), false, false);
  return fmaxf(__uint_as_float(t[0]), __uint_as_float(t[1]));
}
__device__ __forceinline__ float xrow16_sum(float x) {
  auto s = __builtin_amdgcn_permlane16_swap(__float_as_uint(x), __float_as_uint(x), false, false);
  x = __uint_as_float(s[0]) + __uint_as_float(s[1]);
  auto t = __builtin_amdgcn_permlane32_swap(__float_as_uint(x), __float_as_uint(x), false, false);
  return __uint_as_float(t[0]) + __uint_as_float(t[1]);
}

__device__ __forceinline__ float wave_sum(float x) {
  x += dpp<XOR1>(x); x += dpp<XOR2>(x); x += dpp<XOR7>(x); x += dpp<XOR8>(x);
  return xrow16_sum(x);
}
__device__ __forceinline__ float sum_rg8(float x) { x += dpp<XOR8>(x); return xrow16_sum(x); }
__device__ __forceinline__ float sum_rg16(float x) { x += dpp<ROR4>(x); x += dpp<XOR8>(x); return xrow16_sum(x); }

__device__ __forceinline__ void layernorm_row(const u32x4 v, float* xnT, int b, int lane) {
  float f[8];
#pragma unroll
  for (int k = 0; k < 4; ++k) { f[2 * k] = bflo(v[k]); f[2 * k + 1] = bfhi(v[k]); }
  float s = ((f[0] + f[1]) + (f[2] + f[3])) + ((f[4] + f[5]) + (f[6] + f[7]));
  const float mean = wave_sum(s) * (1.f / DM);
  float q = 0.f;
#pragma unroll
  for (int k = 0; k < 8; ++k) { const float d = f[k] - mean; q = fmaf(d, d, q); }
  const float var = bfr(wave_sum(q) * (1.f / DM));
  const float mb = bfr(mean);
  const float den = bfr(sqrtf(bfr(var + 1e-5f)));
#pragma unroll
  for (int k = 0; k < 8; ++k) xnT[(lane * 8 + k) * 8 + b] = bfr(bfr(f[k] - mb) / den);
}

typedef __amdgpu_buffer_rsrc_t rsrc_t;
__device__ __forceinline__ rsrc_t mk_rsrc(const void* p, u32 bytes) { return __builtin_amdgcn_make_buffer_rsrc(const_cast<void*>(p), 0, bytes, 0x00020000); }
__device__ __forceinline__ u32x4 ld16_sc1(rsrc_t r, u32 off) { return __builtin_bit_cast(u32x4, __builtin_amdgcn_raw_buffer_load_b128(r, off, 0, 16)); }
__device__ __forceinline__ void st4_sc1(void* p, u32 v) { __hip_atomic_store(reinterpret_cast<u32*>(p), v, __ATOMIC_RELAXED, __HIP_MEMORY_SCOPE_AGENT); }
__device__ __forceinline__ void st8_sc1(void* p, float a, float b) {
  __hip_atomic_store(reinterpret_cast<unsigned long long*>(p), (unsigned long long)__float_as_uint(a) | ((unsigned long long)__float_as_uint(b) << 32), __ATOMIC_RELAXED, __HIP_MEMORY_SCOPE_AGENT);
}
__device__ __forceinline__ float ld4_sc1(const float* p) { return __hip_atomic_load(p, __ATOMIC_RELAXED, __HIP_MEMORY_SCOPE_AGENT); }
#define DRAIN() asm volatile("s_waitcnt vmcnt(0)" ::: "memory")
__device__ __forceinline__ void arrive_and_wait(u32* cnt, u32 target) {
  u32 v = __hip_atomic_fetch_add(cnt, 1u, __ATOMIC_RELAXED, __HIP_MEMORY_SCOPE_AGENT) + 1u;
  for (int spin = 0; v < target && spin < (1 << 22); ++spin) {
    __builtin_amdgcn_s_sleep(2);
    v = __hip_atomic_load(cnt, __ATOMIC_RELAXED, __HIP_MEMORY_SCOPE_AGENT);
  }
}

__global__ void __launch_bounds__(64) k_zero(unsigned char* __restrict__ ws) {
  if (threadIdx.x < 48) reinterpret_cast<u32*>(ws + WS_CNT)[threadIdx.x * 32] = 0u;
}

constexpr int AW = 8;
constexpr int TPW = KPS / (16 * AW);
constexpr int MRG_REC = 4 + DH;
constexpr int L_XNT = 0;
constexpr int L_RED = L_XNT + DM * 8;
constexpr int L_P = L_RED + 2048;
constexpr int L_MRG = L_P + AW * 64;
constexpr int L_HT = L_MRG + AW * 4 * MRG_REC;
constexpr int L_FLAG = L_HT + 224 * 8;
constexpr int L_TOTAL = L_FLAG + 4;

__global__ void __launch_bounds__(512) k_fused(const u16* __restrict__ x, const u16* __restrict__ Kc_, const u16* __restrict__ Vc_,
                                               const u16* __restrict__ wq, const u16* __restrict__ wo, const u16* __restrict__ w1,
                                               const u16* __restrict__ b1, const u16* __restrict__ w2, const u16* __restrict__ b2,
                                               unsigned char* __restrict__ ws, u16* __restrict__ out) {
  __shared__ __attribute__((aligned(16))) float lds[L_TOTAL];
  const int tid = threadIdx.x, lane = tid & 63, wave = __builtin_amdgcn_readfirstlane(tid >> 6), r = lane >> 4, c = lane & 15;
  const int kvh = blockIdx.x & 7, jj = blockIdx.x >> 3, b = jj >> 2, s = jj & 3;
  constexpr float SCALE = 0.08838834764831845f, LOG2E = 1.4426950408889634f;
  u32* cnt = reinterpret_cast<u32*>(ws + WS_CNT);
  const u32x4 xv4 = *reinterpret_cast<const u32x4*>(x + wave * DM + lane * 8);
  __builtin_amdgcn_sched_barrier(0);
  const int jh = jj < 28 ? jj : 27;
  const int colh = kvh * 224 + jh * 8;
  const u32 b1v = *reinterpret_cast<const u32*>(b1 + colh + 2 * (tid & 3));
  const u16* wqp = wq + (size_t)(wave * 64 + (lane >> 3)) * (HQ * DH) + kvh * 512 + jj * 16 + 2 * (lane & 7);
  u32 wqv[8];
#pragma unroll
  for (int i = 0; i < 8; ++i) wqv[i] = *reinterpret_cast<const u32*>(wqp + (size_t)(8 * i) * (HQ * DH));
  const u16* w1p = w1 + (size_t)(wave * 64 + (lane >> 2)) * FF + colh + 2 * (lane & 3);
  u32 w1v[4];
#pragma unroll
  for (int i = 0; i < 4; ++i) w1v[i] = *reinterpret_cast<const u32*>(w1p + (size_t)(16 * i) * FF);
  __builtin_amdgcn_sched_barrier(0);
  const size_t base = ((size_t)(b * HKV + kvh) * SK + (size_t)s * KPS + wave * 16 + r) * DH + 8 * c;
  const u16* kb = Kc_ + base;
  const u16* vb = Vc_ + base;
  u32x4 Kr[3][4], Vr[3][4];
#define ISSUE(i_) do { _Pragma("unroll") for (int t_ = 0; t_ < 4; ++t_) { \
    Kr[(i_) % 3][t_] = __builtin_nontemporal_load(reinterpret_cast<const u32x4*>(kb + (size_t)((i_) * AW * 16 + t_ * 4) * DH)); \
    Vr[(i_) % 3][t_] = __builtin_nontemporal_load(reinterpret_cast<const u32x4*>(vb + (size_t)((i_) * AW * 16 + t_ * 4) * DH)); } } while (0)
  ISSUE(0);
  ISSUE(1);
  ISSUE(2);
  __builtin_amdgcn_sched_barrier(0);
  const int fo = tid & 127;
  const u16 xin = x[(fo >> 4) * DM + jj * 16 + (fo & 15)], b2in = b2[jj * 16 + (fo & 15)];
  const u16* wop = wo + (size_t)(kvh * 512 + wave * 64 + (lane >> 3)) * DM + jj * 16 + 2 * (lane & 7);
  u32 wov[8], w2v[4];
#pragma unroll
  for (int i = 0; i < 8; ++i) wov[i] = *reinterpret_cast<const u32*>(wop + (size_t)(8 * i) * DM);
  const u16* w2p = w2 + (size_t)(kvh * 224 + wave * 28) * DM + jj * 16 + 2 * (lane & 7);
#pragma unroll
  for (int i = 0; i < 4; ++i) { const int rr = (lane >> 3) + 8 * i; w2v[i] = rr < 28 ? *reinterpret_cast<const u32*>(w2p + (size_t)rr * DM) : 0u; }
  __builtin_amdgcn_sched_barrier(0);
  {
    float* xnT = lds + L_XNT;
    float* red = lds + L_RED;
    layernorm_row(xv4, xnT, wave, lane);
    __syncthreads();
    float acc[16], ah[16];
#pragma unroll
    for (int i = 0; i < 16; ++i) { acc[i] = 0.f; ah[i] = 0.f; }
#pragma unroll
    for (int i = 0; i < 8; ++i) {
      const int row = wave * 64 + (lane >> 3) + 8 * i;
      const f32x4 xa = *reinterpret_cast<const f32x4*>(xnT + row * 8), xb = *reinterpret_cast<const f32x4*>(xnT + row * 8 + 4);
      const float wl = bflo(wqv[i]), wh = bfhi(wqv[i]);
#pragma unroll
      for (int bb = 0; bb < 4; ++bb) {
        acc[2 * bb] = fmaf(xa[bb], wl, acc[2 * bb]); acc[2 * bb + 1] = fmaf(xa[bb], wh, acc[2 * bb + 1]);
        acc[8 + 2 * bb] = fmaf(xb[bb], wl, acc[8 + 2 * bb]); acc[8 + 2 * bb + 1] = fmaf(xb[bb], wh, acc[8 + 2 * bb + 1]);
      }
    }
#pragma unroll
    for (int i = 0; i < 4; ++i) {
      const int row = wave * 64 + (lane >> 2) + 16 * i;
      const f32x4 xa = *reinterpret_cast<const f32x4*>(xnT + row * 8), xb = *reinterpret_cast<const f32x4*>(xnT + row * 8 + 4);
      const float wl = bflo(w1v[i]), wh = bfhi(w1v[i]);
#pragma unroll
      for (int bb = 0; bb < 4; ++bb) {
        ah[2 * bb] = fmaf(xa[bb], wl, ah[2 * bb]); ah[2 * bb + 1] = fmaf(xa[bb], wh, ah[2 * bb + 1]);
        ah[8 + 2 * bb] = fmaf(xb[bb], wl, ah[8 + 2 * bb]); ah[8 + 2 * bb + 1] = fmaf(xb[bb], wh, ah[8 + 2 * bb + 1]);
      }
    }
#pragma unroll
    for (int i = 0; i < 16; ++i) { acc[i] = sum_rg8(acc[i]); ah[i] = sum_rg16(ah[i]); }
    if (lane < 8) {
#pragma unroll
      for (int i = 0; i < 16; i += 4) *reinterpret_cast<f32x4*>(red + (wave * 8 + lane) * 16 + i) = f32x4{acc[i], acc[i + 1], acc[i + 2], acc[i + 3]};
    } else if (lane < 12) {
#pragma unroll
      for (int i = 0; i < 16; i += 4) *reinterpret_cast<f32x4*>(red + 1024 + (wave * 4 + lane - 8) * 16 + i) = f32x4{ah[i], ah[i + 1], ah[i + 2], ah[i + 3]};
    }
    __syncthreads();
    if (tid < 64) {
      const int bb = tid >> 3, cl = tid & 7;
      float s0 = 0.f, s1 = 0.f;
#pragma unroll
      for (int w = 0; w < 8; ++w) { const f32x2 t = *reinterpret_cast<const f32x2*>(red + (w * 8 + cl) * 16 + 2 * bb); s0 += t[0]; s1 += t[1]; }
      st4_sc1(reinterpret_cast<u16*>(ws + WS_Q) + bb * (HQ * DH) + kvh * 512 + jj * 16 + 2 * cl, (u32)f2bf(s0) | ((u32)f2bf(s1) << 16));
      DRAIN();
    } else if (tid < 96) {
      const int t = tid - 64, bb = t >> 2, cl = t & 3;
      float s0 = 0.f, s1 = 0.f;
#pragma unroll
      for (int w = 0; w < 8; ++w) { const f32x2 tt = *reinterpret_cast<const f32x2*>(red + 1024 + (w * 4 + cl) * 16 + 2 * bb); s0 += tt[0]; s1 += tt[1]; }
      const float h0 = bfr(bfr(s0) + bflo(b1v)), h1 = bfr(bfr(s1) + bfhi(b1v));
      const float g0 = 0.5f * h0 * (1.f + tanhf(0.7978845608028654f * (h0 + 0.044715f * h0 * h0 * h0)));
      const float g1 = 0.5f * h1 * (1.f + tanhf(0.7978845608028654f * (h1 + 0.044715f * h1 * h1 * h1)));
      if (jj < 28) st4_sc1(reinterpret_cast<u16*>(ws + WS_H) + bb * FF + colh + 2 * cl, (u32)f2bf(g0) | ((u32)f2bf(g1) << 16));
      DRAIN();
    }
    __syncthreads();
    if (tid == 0) arrive_and_wait(cnt + kvh * 32, 32u);
    __syncthreads();
  }
  u32x4 qpk[4];
  {
    const rsrc_t rq = mk_rsrc(ws + WS_Q, NB * HQ * DH * 2);
#pragma unroll
    for (int g = 0; g < 4; ++g) qpk[g] = ld16_sc1(rq, (u32)((b * (HQ * DH) + (kvh * 4 + g) * DH + 8 * c) * 2));
  }
  float m = -INFINITY, l = 0.f, acc[32];
#pragma unroll
  for (int k = 0; k < 32; ++k) acc[k] = 0.f;
#pragma unroll
  for (int i = 0; i < TPW; ++i) {
    float xs[16];
#pragma unroll
    for (int t = 0; t < 4; ++t)
#pragma unroll
      for (int g = 0; g < 4; ++g) {
        float d = 0.f;
#pragma unroll
        for (int k = 0; k < 4; ++k) d = dot2(Kr[i % 3][t][k], qpk[g][k], d);
        xs[t * 4 + g] = d;
      }
#define TR_STEP(HALF, CTRL, BIT) _Pragma("unroll") for (int h = 0; h < (HALF); ++h) { \
      const float lo_ = xs[h], hi_ = xs[h + (HALF)]; const float t1_ = lo_ + dpp<CTRL>(lo_), t2_ = hi_ + dpp<CTRL>(hi_); \
      xs[h] = (c & (BIT)) ? t2_ : t1_; }
    TR_STEP(8, XOR8, 8) TR_STEP(4, XOR7, 4) TR_STEP(2, XOR2, 2) TR_STEP(1, XOR1, 1)
#undef TR_STEP
    const float sc = bfr(bfr(xs[0]) * SCALE) * LOG2E;
    float pm = sc; pm = fmaxf(pm, dpp<ROR4>(pm)); pm = fmaxf(pm, dpp<XOR8>(pm)); pm = xrow16_max(pm);
    const float mn = fmaxf(m, pm);
    const float alpha = __builtin_amdgcn_exp2f(m - mn), p = __builtin_amdgcn_exp2f(sc - mn);
    l = fmaf(l, alpha, p); m = mn;
    {
      const float a0 = dpp<0x00>(alpha), a1 = dpp<0x55>(alpha), a2 = dpp<0xAA>(alpha), a3 = dpp<0xFF>(alpha);
#pragma unroll
      for (int k = 0; k < 8; ++k) { acc[k] *= a0; acc[8 + k] *= a1; acc[16 + k] *= a2; acc[24 + k] *= a3; }
    }
    float* prow = lds + L_P + (wave * 4 + r) * 16;
    prow[c] = p;
    asm volatile("s_waitcnt lgkmcnt(0)" ::: "memory");
    f32x4 pr[4];
#pragma unroll
    for (int u = 0; u < 4; ++u) pr[u] = *reinterpret_cast<const f32x4*>(prow + 4 * u);
#pragma unroll
    for (int t = 0; t < 4; ++t) {
      float vf[8];
#pragma unroll
      for (int k = 0; k < 4; ++k) { vf[2 * k] = bflo(Vr[i % 3][t][k]); vf[2 * k + 1] = bfhi(Vr[i % 3][t][k]); }
#pragma unroll
      for (int g = 0; g < 4; ++g) {
#pragma unroll
        for (int k = 0; k < 8; ++k) acc[g * 8 + k] = fmaf(pr[t][g], vf[k], acc[g * 8 + k]);
      }
    }
    if (i + 3 < TPW) ISSUE(i + 3);
  }
#undef ISSUE
#pragma unroll
  for (int k = 0; k < 32; ++k) acc[k] = xrow16_sum(acc[k]);
  { float t = l; t += dpp<ROR4>(t); t += dpp<XOR8>(t); l = xrow16_sum(t); }
  float* mrg = lds + L_MRG;
  if (r == 0) {
#pragma unroll
    for (int g = 0; g < 4; ++g) {
      float* rec = mrg + (wave * 4 + g) * MRG_REC;
#pragma unroll
      for (int k = 0; k < 8; k += 4) *reinterpret_cast<f32x4*>(rec + 4 + 8 * c + k) = f32x4{acc[g * 8 + k], acc[g * 8 + k + 1], acc[g * 8 + k + 2], acc[g * 8 + k + 3]};
      if (c == g) { rec[0] = m; rec[1] = l; }
    }
  }
  __syncthreads();
  if (wave < 4) {
    const int g = wave, d0 = 2 * lane;
    float M = -INFINITY;
#pragma unroll
    for (int w = 0; w < AW; ++w) M = fmaxf(M, mrg[(w * 4 + g) * MRG_REC]);
    float L = 0.f, o0 = 0.f, o1 = 0.f;
#pragma unroll
    for (int w = 0; w < AW; ++w) {
      const float* rec = mrg + (w * 4 + g) * MRG_REC;
      const float wt = __builtin_amdgcn_exp2f(rec[0] - M);
      L = fmaf(wt, rec[1], L); o0 = fmaf(wt, rec[4 + d0], o0); o1 = fmaf(wt, rec[4 + d0 + 1], o1);
    }
    const size_t slot = (size_t)(b * HQ + kvh * 4 + g) * NSPLIT + s;
    st8_sc1(reinterpret_cast<float*>(ws + WS_PO) + slot * DH + d0, o0, o1);
    if (lane == 0) st8_sc1(reinterpret_cast<float*>(ws + WS_PML) + slot * 2, M, L);
    DRAIN();
  }
  __syncthreads();
  if (tid == 0) arrive_and_wait(cnt + (8 + kvh) * 32, 32u);
  __syncthreads();
  float* oT = lds + L_XNT;
  float* hT = lds + L_HT;
  float* red = lds + L_RED;
  {
    const int g = lane >> 4, d0 = (lane & 15) * 8;
    const u32 slot = (u32)(wave * HQ + kvh * 4 + g) * NSPLIT;
    const rsrc_t rp = mk_rsrc(ws + WS_PML, (u32)(WS_PART - WS_PML));
    const rsrc_t rh = mk_rsrc(ws + WS_H, NB * FF * 2);
    u32x4 ml[NSPLIT / 2], a0[NSPLIT], a1[NSPLIT];
#pragma unroll
    for (int ss = 0; ss < NSPLIT / 2; ++ss) ml[ss] = ld16_sc1(rp, slot * 8 + 16 * ss);
#pragma unroll
    for (int ss = 0; ss < NSPLIT; ++ss) {
      a0[ss] = ld16_sc1(rp, (u32)(WS_PO - WS_PML) + ((slot + ss) * DH + d0) * 4);
      a1[ss] = ld16_sc1(rp, (u32)(WS_PO - WS_PML) + ((slot + ss) * DH + d0) * 4 + 16);
    }
    const int ht = tid < 224 ? tid : 223;
    const u32x4 hv = ld16_sc1(rh, (u32)(((ht / 28) * FF + kvh * 224 + (ht % 28) * 8) * 2));
    float M = -INFINITY;
#pragma unroll
    for (int ss = 0; ss < NSPLIT; ++ss) M = fmaxf(M, __uint_as_float(ml[ss >> 1][(ss & 1) * 2]));
    float L = 0.f; f32x4 o0 = {0.f, 0.f, 0.f, 0.f}, o1 = {0.f, 0.f, 0.f, 0.f};
#pragma unroll
    for (int ss = 0; ss < NSPLIT; ++ss) {
      const float w = __builtin_amdgcn_exp2f(__uint_as_float(ml[ss >> 1][(ss & 1) * 2]) - M);
      L = fmaf(w, __uint_as_float(ml[ss >> 1][(ss & 1) * 2 + 1]), L);
      o0 += w * __builtin_bit_cast(f32x4, a0[ss]); o1 += w * __builtin_bit_cast(f32x4, a1[ss]);
    }
    const float inv = 1.f / L;
#pragma unroll
    for (int k = 0; k < 4; ++k) { oT[(g * DH + d0 + k) * 8 + wave] = bfr(o0[k] * inv); oT[(g * DH + d0 + 4 + k) * 8 + wave] = bfr(o1[k] * inv); }
    if (tid < 224) {
      const int bb = tid / 28, r8 = (tid % 28) * 8;
#pragma unroll
      for (int k = 0; k < 4; ++k) { hT[(r8 + 2 * k) * 8 + bb] = bflo(hv[k]); hT[(r8 + 2 * k + 1) * 8 + bb] = bfhi(hv[k]); }
    }
  }
  __syncthreads();
  {
    float ao[16], ah[16];
#pragma unroll
    for (int i = 0; i < 16; ++i) { ao[i] = 0.f; ah[i] = 0.f; }
#pragma unroll
    for (int i = 0; i < 8; ++i) {
      const int row = wave * 64 + (lane >> 3) + 8 * i;
      const f32x4 xa = *reinterpret_cast<const f32x4*>(oT + row * 8), xb = *reinterpret_cast<const f32x4*>(oT + row * 8 + 4);
      const float wl = bflo(wov[i]), wh = bfhi(wov[i]);
#pragma unroll
      for (int bb = 0; bb < 4; ++bb) {
        ao[2 * bb] = fmaf(xa[bb], wl, ao[2 * bb]); ao[2 * bb + 1] = fmaf(xa[bb], wh, ao[2 * bb + 1]);
        ao[8 + 2 * bb] = fmaf(xb[bb], wl, ao[8 + 2 * bb]); ao[8 + 2 * bb + 1] = fmaf(xb[bb], wh, ao[8 + 2 * bb + 1]);
      }
    }
#pragma unroll
    for (int i = 0; i < 4; ++i) {
      const int rr = (lane >> 3) + 8 * i, row = wave * 28 + (rr < 28 ? rr : 0);
      const f32x4 xa = *reinterpret_cast<const f32x4*>(hT + row * 8), xb = *reinterpret_cast<const f32x4*>(hT + row * 8 + 4);
      const float wl = bflo(w2v[i]), wh = bfhi(w2v[i]);
#pragma unroll
      for (int bb = 0; bb < 4; ++bb) {
        ah[2 * bb] = fmaf(xa[bb], wl, ah[2 * bb]); ah[2 * bb + 1] = fmaf(xa[bb], wh, ah[2 * bb + 1]);
        ah[8 + 2 * bb] = fmaf(xb[bb], wl, ah[8 + 2 * bb]); ah[8 + 2 * bb + 1] = fmaf(xb[bb], wh, ah[8 + 2 * bb + 1]);
      }
    }
#pragma unroll
    for (int i = 0; i < 16; ++i) { ao[i] = sum_rg8(ao[i]); ah[i] = sum_rg8(ah[i]); }
    if (lane < 8) {
#pragma unroll
      for (int i = 0; i < 16; i += 4) {
        *reinterpret_cast<f32x4*>(red + (wave * 8 + lane) * 32 + i) = f32x4{ao[i], ao[i + 1], ao[i + 2], ao[i + 3]};
        *reinterpret_cast<f32x4*>(red + (wave * 8 + lane) * 32 + 16 + i) = f32x4{ah[i], ah[i + 1], ah[i + 2], ah[i + 3]};
      }
    }
  }
  __syncthreads();
  float* part = reinterpret_cast<float*>(ws + WS_PART);
  if (tid < 256) {
    const int which = tid >> 7, bb = (tid >> 4) & 7, cc = tid & 15;
    float sum = 0.f;
#pragma unroll
    for (int w = 0; w < 8; ++w) sum += red[(w * 8 + (cc >> 1)) * 32 + which * 16 + 2 * bb + (cc & 1)];
    st4_sc1(part + (size_t)(jj * 8 + kvh) * 256 + tid, __float_as_uint(sum));
    DRAIN();
  }
  __syncthreads();
  if (tid == 0) {
    const u32 old = __hip_atomic_fetch_add(cnt + (16 + jj) * 32, 1u, __ATOMIC_RELAXED, __HIP_MEMORY_SCOPE_AGENT);
    reinterpret_cast<u32*>(lds + L_FLAG)[0] = (old == 7u) ? 1u : 0u;
  }
  __syncthreads();
  if (reinterpret_cast<const u32*>(lds + L_FLAG)[0] == 0u) return;
  if (tid < 128) {
    float sv[8], gv[8];
#pragma unroll
    for (int xx = 0; xx < 8; ++xx) {
      sv[xx] = ld4_sc1(part + (size_t)(jj * 8 + xx) * 256 + tid);
      gv[xx] = ld4_sc1(part + (size_t)(jj * 8 + xx) * 256 + 128 + tid);
    }
    float ss = 0.f, gg = 0.f;
#pragma unroll
    for (int xx = 0; xx < 8; ++xx) { ss += sv[xx]; gg += gv[xx]; }
    const float sb = bfr(ss);
    const float f = bfr(bfr(gg) + bf2f(b2in));
    const float gt = bfr(f / (1.f + __expf(-f)));
    out[(tid >> 4) * DM + jj * 16 + (tid & 15)] = f2bf(bf2f(xin) + bfr(sb * gt));
  }
}

extern "C" void kernel_launch(void* const* d_in, const int* in_sizes, int n_in,
                              void* d_out, int out_size, void* d_ws, size_t ws_size,
                              hipStream_t stream) {
  const u16* x = (const u16*)d_in[0];
  const u16* kc = (const u16*)d_in[1];
  const u16* vc = (const u16*)d_in[2];
  const u16* wq = (const u16*)d_in[3];
  const u16* wo = (const u16*)d_in[4];
  const u16* w1 = (const u16*)d_in[6];
  const u16* b1 = (const u16*)d_in[7];
  const u16* w2 = (const u16*)d_in[8];
  const u16* b2 = (const u16*)d_in[9];
  unsigned char* ws = (unsigned char*)d_ws;
  k_zero<<<1, 64, 0, stream>>>(ws);
  k_fused<<<256, 512, 0, stream>>>(x, kc, vc, wq, wo, w1, b1, w2, b2, ws, (u16*)d_out);
}
```

```cpp
#include <hip/hip_runtime.h>
#include <hip/hip_bf16.h>
#include <stdint.h>

typedef unsigned int u32;
typedef unsigned short u16;
typedef __attribute__((ext_vector_type(4))) unsigned u32x4;
typedef __attribute__((ext_vector_type(4))) float f32x4;
typedef __attribute__((ext_vector_type(2))) float f32x2;
typedef __attribute__((ext_vector_type(2))) __bf16 bf16x2;

constexpr int NB = 8, DM = 512, HQ = 32, HKV = 8, SK = 4096, DH = 128, FF = 1792;
constexpr int NSPLIT = 4;
constexpr int KPS = SK / NSPLIT;

constexpr size_t WS_CNT = 0;
constexpr size_t WS_Q = 8192;
constexpr size_t WS_H = WS_Q + NB * HQ * DH * 2;
constexpr size_t WS_PML = WS_H + 32768;
constexpr size_t WS_PO = WS_PML + NB * HQ * NSPLIT * 2 * 4;
constexpr size_t WS_PART = WS_PO + (size_t)NB * HQ * NSPLIT * DH * 4;

__device__ __forceinline__ float bf2f(u16 v) { return __uint_as_float(((u32)v) << 16); }
__device__ __forceinline__ float bflo(u32 v) { return __uint_as_float(v << 16); }
__device__ __forceinline__ float bfhi(u32 v) { return __uint_as_float(v & 0xffff0000u); }
__device__ __forceinline__ u16 f2bf(float f) { return __builtin_bit_cast(u16, (__bf16)f); }
__device__ __forceinline__ float bfr(float f) { return (float)(__bf16)f; }

__device__ __forceinline__ float dot2(u32 k, u32 q, float acc) {
  return __builtin_amdgcn_fdot2_f32_bf16(__builtin_bit_cast(bf16x2, k), __builtin_bit_cast(bf16x2, q), acc, false);
}
template <int CTRL> __device__ __forceinline__ float dpp(float x) {
  return __builtin_bit_cast(float, __builtin_amdgcn_mov_dpp(__builtin_bit_cast(int, x), CTRL, 0xf, 0xf, true));
}
constexpr int XOR1 = 0xB1, XOR2 = 0x4E, XOR7 = 0x141, XOR8 = 0x128, ROR4 = 0x124;
__device__ __forceinline__ float xrow16_max(float x) {
  auto s = __builtin_amdgcn_permlane16_swap(__float_as_uint(x), __float_as_uint(x), false, false);
  x = fmaxf(__uint_as_float(s[0]), __uint_as_float(s[1]));
  auto t = __builtin_amdgcn_permlane32_swap(__float_as_uint(x), __float_as_uint(x), false, false);
  return fmaxf(__uint_as_float(t[0]), __uint_as_float(t[1]));
}
__device__ __forceinline__ float xrow16_sum(float x) {
  auto s = __builtin_amdgcn_permlane16_swap(__float_as_uint(x), __float_as_uint(x), false, false);
  x = __uint_as_float(s[0]) + __uint_as_float(s[1]);
  auto t = __builtin_amdgcn_permlane32_swap(__float_as_uint(x), __float_as_uint(x), false, false);
  return __uint_as_float(t[0]) + __uint_as_float(t[1]);
}

__device__ __forceinline__ float wave_sum(float x) {
  x += dpp<XOR1>(x); x += dpp<XOR2>(x); x += dpp<XOR7>(x); x += dpp<XOR8>(x);
  return xrow16_sum(x);
}
__device__ __forceinline__ float sum_rg8(float x) { x += dpp<XOR8>(x); return xrow16_sum(x); }
__device__ __forceinline__ float sum_rg16(float x) { x += dpp<ROR4>(x); x += dpp<XOR8>(x); return xrow16_sum(x); }

typedef __attribute__((ext_vector_type(8))) __bf16 bf16x8;
__device__ __forceinline__ f32x4 mfma16(u32x4 a, u32x4 b, f32x4 c) {
  return __builtin_amdgcn_mfma_f32_16x16x32_bf16(__builtin_bit_cast(bf16x8, a), __builtin_bit_cast(bf16x8, b), c, 0, 0, 0);
}
__device__ __forceinline__ u32x4 ld_bfrag(const u16* p, size_t ld) {
  u32x4 r;
#pragma unroll
  for (int i = 0; i < 4; ++i) r[i] = (u32)p[(2 * i) * ld] | ((u32)p[(2 * i + 1) * ld] << 16);
  return r;
}
__device__ __forceinline__ u32 pk2(float a, float b) { return (u32)f2bf(a) | ((u32)f2bf(b) << 16); }

typedef __amdgpu_buffer_rsrc_t rsrc_t;
__device__ __forceinline__ rsrc_t mk_rsrc(const void* p, u32 bytes) { return __builtin_amdgcn_make_buffer_rsrc(const_cast<void*>(p), 0, bytes, 0x00020000); }
__device__ __forceinline__ u32x4 ld16_sc1(rsrc_t r, u32 off) { return __builtin_bit_cast(u32x4, __builtin_amdgcn_raw_buffer_load_b128(r, off, 0, 16)); }
__device__ __forceinline__ void st4_sc1(void* p, u32 v) { __hip_atomic_store(reinterpret_cast<u32*>(p), v, __ATOMIC_RELAXED, __HIP_MEMORY_SCOPE_AGENT); }
__device__ __forceinline__ void st8_sc1(void* p, float a, float b) {
  __hip_atomic_store(reinterpret_cast<unsigned long long*>(p), (unsigned long long)__float_as_uint(a) | ((unsigned long long)__float_as_uint(b) << 32), __ATOMIC_RELAXED, __HIP_MEMORY_SCOPE_AGENT);
}
__device__ __forceinline__ float ld4_sc1(const float* p) { return __hip_atomic_load(p, __ATOMIC_RELAXED, __HIP_MEMORY_SCOPE_AGENT); }
#define DRAIN() asm volatile("s_waitcnt vmcnt(0)" ::: "memory")
__device__ __forceinline__ void arrive_and_wait(u32* cnt, u32 target) {
  u32 v = __hip_atomic_fetch_add(cnt, 1u, __ATOMIC_RELAXED, __HIP_MEMORY_SCOPE_AGENT) + 1u;
  for (int spin = 0; v < target && spin < (1 << 16); ++spin) {
    __builtin_amdgcn_s_sleep(1);
    v = __hip_atomic_load(cnt, __ATOMIC_RELAXED, __HIP_MEMORY_SCOPE_AGENT);
  }
}

__global__ void __launch_bounds__(512) k_prep(const u16* __restrict__ x, const u16* __restrict__ wq, const u16* __restrict__ w1,
                                              const u16* __restrict__ b1, unsigned char* __restrict__ ws) {
  __shared__ __attribute__((aligned(16))) u16 xnb[NB * DM];
  __shared__ __attribute__((aligned(16))) float red[8 * 2 * 128];
  const int tid = threadIdx.x, lane = tid & 63, wave = tid >> 6, g = lane >> 4, n = lane & 15;
  const int kvh = blockIdx.x & 7, jj = blockIdx.x >> 3;
  const u32x4 xv = *reinterpret_cast<const u32x4*>(x + wave * DM + lane * 8);
  const int jh = jj < 28 ? jj : 27;
  const int colh = kvh * 224 + jh * 8;
  const u16 b1v = b1[colh + (tid & 7)];
  __builtin_amdgcn_sched_barrier(0);
  const u16* wqp = wq + (size_t)(wave * 64 + 8 * g) * (HQ * DH) + kvh * 512 + jj * 16 + n;
  const u32x4 bq0 = ld_bfrag(wqp, HQ * DH), bq1 = ld_bfrag(wqp + (size_t)32 * (HQ * DH), HQ * DH);
  const u16* w1p = w1 + (size_t)(wave * 64 + 8 * g) * FF + colh + (n & 7);
  const u32x4 bh0 = ld_bfrag(w1p, FF), bh1 = ld_bfrag(w1p + (size_t)32 * FF, FF);
  if (blockIdx.x == 0 && tid < 48) reinterpret_cast<u32*>(ws + WS_CNT)[tid * 32] = 0u;
  __builtin_amdgcn_sched_barrier(0);
  {
    float f[8];
#pragma unroll
    for (int k = 0; k < 4; ++k) { f[2 * k] = bflo(xv[k]); f[2 * k + 1] = bfhi(xv[k]); }
    const float sm = ((f[0] + f[1]) + (f[2] + f[3])) + ((f[4] + f[5]) + (f[6] + f[7]));
    const float mean = wave_sum(sm) * (1.f / DM);
    float q = 0.f;
#pragma unroll
    for (int k = 0; k < 8; ++k) { const float d = f[k] - mean; q = fmaf(d, d, q); }
    const float var = bfr(wave_sum(q) * (1.f / DM));
    const float mb = bfr(mean);
    const float den = bfr(sqrtf(bfr(var + 1e-5f)));
    u32x4 o;
#pragma unroll
    for (int k = 0; k < 4; ++k) o[k] = pk2(bfr(f[2 * k] - mb) / den, bfr(f[2 * k + 1] - mb) / den);
    *reinterpret_cast<u32x4*>(xnb + wave * DM + lane * 8) = o;
  }
  __syncthreads();
  u32x4 a0 = {0u, 0u, 0u, 0u}, a1 = {0u, 0u, 0u, 0u};
  if (n < 8) { a0 = *reinterpret_cast<const u32x4*>(xnb + n * DM + wave * 64 + 8 * g); a1 = *reinterpret_cast<const u32x4*>(xnb + n * DM + wave * 64 + 32 + 8 * g); }
  f32x4 cq = {0.f, 0.f, 0.f, 0.f}, chh = {0.f, 0.f, 0.f, 0.f};
  cq = mfma16(a0, bq0, cq); chh = mfma16(a0, bh0, chh);
  cq = mfma16(a1, bq1, cq); chh = mfma16(a1, bh1, chh);
  if (g < 2) {
#pragma unroll
    for (int j = 0; j < 4; ++j) { red[(wave * 2 + 0) * 128 + (4 * g + j) * 16 + n] = cq[j]; red[(wave * 2 + 1) * 128 + (4 * g + j) * 16 + n] = chh[j]; }
  }
  __syncthreads();
  if (tid < 256) {
    const int which = tid >> 7, t = tid & 127, bb = t >> 4, nn = t & 15;
    float sum = 0.f;
#pragma unroll
    for (int w = 0; w < 8; ++w) sum += red[(w * 2 + which) * 128 + t];
    if (which == 0) {
      reinterpret_cast<u16*>(ws + WS_Q)[bb * (HQ * DH) + kvh * 512 + jj * 16 + nn] = f2bf(sum);
    } else if (nn < 8 && jj < 28) {
      const float h0 = bfr(bfr(sum) + bf2f(b1v));
      const float g0 = 0.5f * h0 * (1.f + tanhf(0.7978845608028654f * (h0 + 0.044715f * h0 * h0 * h0)));
      reinterpret_cast<u16*>(ws + WS_H)[bb * FF + colh + nn] = f2bf(g0);
    }
  }
}

constexpr int AW = 8;
constexpr int TPW = KPS / (16 * AW);
constexpr int MRG_REC = 4 + DH;
constexpr int L_OB = 0;
constexpr int L_RED = L_OB + 2048;
constexpr int L_P = L_RED + 2048;
constexpr int L_MRG = L_P + AW * 64;
constexpr int L_FLAG = L_MRG + AW * 4 * MRG_REC;
constexpr int L_TOTAL = L_FLAG + 4;

__global__ void __launch_bounds__(512) k_main(const u16* __restrict__ x, const u16* __restrict__ Kc_, const u16* __restrict__ Vc_,
                                              const u16* __restrict__ wo, const u16* __restrict__ w2, const u16* __restrict__ b2,
                                              unsigned char* __restrict__ ws, u16* __restrict__ out) {
  __shared__ __attribute__((aligned(16))) float lds[L_TOTAL];
  const int tid = threadIdx.x, lane = tid & 63, wave = __builtin_amdgcn_readfirstlane(tid >> 6), r = lane >> 4, c = lane & 15;
  const int kvh = blockIdx.x & 7, jj = blockIdx.x >> 3, b = jj >> 2, s = jj & 3;
  constexpr float SCALE = 0.08838834764831845f, LOG2E = 1.4426950408889634f;
  u32* cnt = reinterpret_cast<u32*>(ws + WS_CNT);
  const u16* qg = reinterpret_cast<const u16*>(ws + WS_Q) + b * (HQ * DH) + kvh * 4 * DH + 8 * c;
  u32x4 qpk[4];
#pragma unroll
  for (int g = 0; g < 4; ++g) qpk[g] = *reinterpret_cast<const u32x4*>(qg + g * DH);
  __builtin_amdgcn_sched_barrier(0);
  const size_t base = ((size_t)(b * HKV + kvh) * SK + (size_t)s * KPS + wave * 16 + r) * DH + 8 * c;
  const u16* kb = Kc_ + base;
  const u16* vb = Vc_ + base;
  u32x4 Kr[3][4], Vr[3][4];
#define ISSUE(i_) do { _Pragma("unroll") for (int t_ = 0; t_ < 4; ++t_) { \
    Kr[(i_) % 3][t_] = __builtin_nontemporal_load(reinterpret_cast<const u32x4*>(kb + (size_t)((i_) * AW * 16 + t_ * 4) * DH)); \
    Vr[(i_) % 3][t_] = __builtin_nontemporal_load(reinterpret_cast<const u32x4*>(vb + (size_t)((i_) * AW * 16 + t_ * 4) * DH)); } } while (0)
  ISSUE(0);
  ISSUE(1);
  __builtin_amdgcn_sched_barrier(0);
  const int fo = tid & 127;
  const u16 xin = x[(fo >> 4) * DM + jj * 16 + (fo & 15)], b2in = b2[jj * 16 + (fo & 15)];
  const u16* wop = wo + (size_t)(kvh * 512 + wave * 64 + 8 * r) * DM + jj * 16 + c;
  const u32x4 bo0 = ld_bfrag(wop, DM), bo1 = ld_bfrag(wop + 32 * DM, DM);
  const int w7 = wave < 7 ? wave : 6;
  u32x4 b2f = ld_bfrag(w2 + (size_t)(kvh * 224 + w7 * 32 + 8 * r) * DM + jj * 16 + c, DM);
  u32x4 ahf = *reinterpret_cast<const u32x4*>(reinterpret_cast<const u16*>(ws + WS_H) + (c & 7) * FF + kvh * 224 + w7 * 32 + 8 * r);
  __builtin_amdgcn_sched_barrier(0);
  float m = -INFINITY, l = 0.f, acc[32];
#pragma unroll
  for (int k = 0; k < 32; ++k) acc[k] = 0.f;
#pragma unroll
  for (int i = 0; i < TPW; ++i) {
    if (i + 2 < TPW) ISSUE(i + 2);
    float xs[16];
#pragma unroll
    for (int t = 0; t < 4; ++t)
#pragma unroll
      for (int g = 0; g < 4; ++g) {
        float d = 0.f;
#pragma unroll
        for (int k = 0; k < 4; ++k) d = dot2(Kr[i % 3][t][k], qpk[g][k], d);
        xs[t * 4 + g] = d;
      }
#define TR_STEP(HALF, CTRL, BIT) _Pragma("unroll") for (int h = 0; h < (HALF); ++h) { \
      const float lo_ = xs[h], hi_ = xs[h + (HALF)]; const float t1_ = lo_ + dpp<CTRL>(lo_), t2_ = hi_ + dpp<CTRL>(hi_); \
      xs[h] = (c & (BIT)) ? t2_ : t1_; }
    TR_STEP(8, XOR8, 8) TR_STEP(4, XOR7, 4) TR_STEP(2, XOR2, 2) TR_STEP(1, XOR1, 1)
#undef TR_STEP
    const float sc = bfr(bfr(xs[0]) * SCALE) * LOG2E;
    float pm = sc; pm = fmaxf(pm, dpp<ROR4>(pm)); pm = fmaxf(pm, dpp<XOR8>(pm)); pm = xrow16_max(pm);
    const float mn = fmaxf(m, pm);
    const float alpha = __builtin_amdgcn_exp2f(m - mn), p = __builtin_amdgcn_exp2f(sc - mn);
    l = fmaf(l, alpha, p); m = mn;
    {
      const float a0 = dpp<0x00>(alpha), a1 = dpp<0x55>(alpha), a2 = dpp<0xAA>(alpha), a3 = dpp<0xFF>(alpha);
#pragma unroll
      for (int k = 0; k < 8; ++k) { acc[k] *= a0; acc[8 + k] *= a1; acc[16 + k] *= a2; acc[24 + k] *= a3; }
    }
    float* prow = lds + L_P + (wave * 4 + r) * 16;
    prow[c] = p;
    asm volatile("s_waitcnt lgkmcnt(0)" ::: "memory");
    f32x4 pr[4];
#pragma unroll
    for (int u = 0; u < 4; ++u) pr[u] = *reinterpret_cast<const f32x4*>(prow + 4 * u);
#pragma unroll
    for (int t = 0; t < 4; ++t) {
      float vf[8];
#pragma unroll
      for (int k = 0; k < 4; ++k) { vf[2 * k] = bflo(Vr[i % 3][t][k]); vf[2 * k + 1] = bfhi(Vr[i % 3][t][k]); }
#pragma unroll
      for (int g = 0; g < 4; ++g) {
#pragma unroll
        for (int k = 0; k < 8; ++k) acc[g * 8 + k] = fmaf(pr[t][g], vf[k], acc[g * 8 + k]);
      }
    }
  }
#undef ISSUE
#pragma unroll
  for (int k = 0; k < 32; ++k) acc[k] = xrow16_sum(acc[k]);
  { float t = l; t += dpp<ROR4>(t); t += dpp<XOR8>(t); l = xrow16_sum(t); }
  float* mrg = lds + L_MRG;
  if (r == 0) {
#pragma unroll
    for (int g = 0; g < 4; ++g) {
      float* rec = mrg + (wave * 4 + g) * MRG_REC;
#pragma unroll
      for (int k = 0; k < 8; k += 4) *reinterpret_cast<f32x4*>(rec + 4 + 8 * c + k) = f32x4{acc[g * 8 + k], acc[g * 8 + k + 1], acc[g * 8 + k + 2], acc[g * 8 + k + 3]};
      if (c == g) { rec[0] = m; rec[1] = l; }
    }
  }
  __syncthreads();
  if (wave < 4) {
    const int g = wave, d0 = 2 * lane;
    float M = -INFINITY;
#pragma unroll
    for (int w = 0; w < AW; ++w) M = fmaxf(M, mrg[(w * 4 + g) * MRG_REC]);
    float L = 0.f, o0 = 0.f, o1 = 0.f;
#pragma unroll
    for (int w = 0; w < AW; ++w) {
      const float* rec = mrg + (w * 4 + g) * MRG_REC;
      const float wt = __builtin_amdgcn_exp2f(rec[0] - M);
      L = fmaf(wt, rec[1], L); o0 = fmaf(wt, rec[4 + d0], o0); o1 = fmaf(wt, rec[4 + d0 + 1], o1);
    }
    const size_t slot = (size_t)(b * HQ + kvh * 4 + g) * NSPLIT + s;
    st8_sc1(reinterpret_cast<float*>(ws + WS_PO) + slot * DH + d0, o0, o1);
    if (lane == 0) st8_sc1(reinterpret_cast<float*>(ws + WS_PML) + slot * 2, M, L);
    DRAIN();
  }
  __syncthreads();
  if (tid == 0) arrive_and_wait(cnt + (8 + kvh) * 32, 32u);
  __syncthreads();
  u16* ob = reinterpret_cast<u16*>(lds + L_OB);
  float* red = lds + L_RED;
  {
    const int d0 = c * 8;
    const u32 slot = (u32)(wave * HQ + kvh * 4 + r) * NSPLIT;
    const rsrc_t rp = mk_rsrc(ws + WS_PML, (u32)(WS_PART - WS_PML));
    u32x4 ml[NSPLIT / 2], a0[NSPLIT], a1[NSPLIT];
#pragma unroll
    for (int ss = 0; ss < NSPLIT / 2; ++ss) ml[ss] = ld16_sc1(rp, slot * 8 + 16 * ss);
#pragma unroll
    for (int ss = 0; ss < NSPLIT; ++ss) {
      a0[ss] = ld16_sc1(rp, (u32)(WS_PO - WS_PML) + ((slot + ss) * DH + d0) * 4);
      a1[ss] = ld16_sc1(rp, (u32)(WS_PO - WS_PML) + ((slot + ss) * DH + d0) * 4 + 16);
    }
    float M = -INFINITY;
#pragma unroll
    for (int ss = 0; ss < NSPLIT; ++ss) M = fmaxf(M, __uint_as_float(ml[ss >> 1][(ss & 1) * 2]));
    float L = 0.f; f32x4 o0 = {0.f, 0.f, 0.f, 0.f}, o1 = {0.f, 0.f, 0.f, 0.f};
#pragma unroll
    for (int ss = 0; ss < NSPLIT; ++ss) {
      const float w = __builtin_amdgcn_exp2f(__uint_as_float(ml[ss >> 1][(ss & 1) * 2]) - M);
      L = fmaf(w, __uint_as_float(ml[ss >> 1][(ss & 1) * 2 + 1]), L);
      o0 += w * __builtin_bit_cast(f32x4, a0[ss]); o1 += w * __builtin_bit_cast(f32x4, a1[ss]);
    }
    const float inv = 1.f / L;
    *reinterpret_cast<u32x4*>(ob + wave * 512 + r * DH + d0) = u32x4{pk2(o0[0] * inv, o0[1] * inv), pk2(o0[2] * inv, o0[3] * inv), pk2(o1[0] * inv, o1[1] * inv), pk2(o1[2] * inv, o1[3] * inv)};
  }
  __syncthreads();
  {
    u32x4 a0 = {0u, 0u, 0u, 0u}, a1 = {0u, 0u, 0u, 0u};
    if (c < 8) { a0 = *reinterpret_cast<const u32x4*>(ob + c * 512 + wave * 64 + 8 * r); a1 = *reinterpret_cast<const u32x4*>(ob + c * 512 + wave * 64 + 32 + 8 * r); }
    if (c >= 8 || wave == 7) ahf = u32x4{0u, 0u, 0u, 0u};
    f32x4 co = {0.f, 0.f, 0.f, 0.f}, cg = {0.f, 0.f, 0.f, 0.f};
    co = mfma16(a0, bo0, co); cg = mfma16(ahf, b2f, cg); co = mfma16(a1, bo1, co);
    if (r < 2) {
#pragma unroll
      for (int j = 0; j < 4; ++j) { red[(wave * 2 + 0) * 128 + (4 * r + j) * 16 + c] = co[j]; red[(wave * 2 + 1) * 128 + (4 * r + j) * 16 + c] = cg[j]; }
    }
  }
  __syncthreads();
  float* part = reinterpret_cast<float*>(ws + WS_PART);
  if (tid < 256) {
    float sum = 0.f;
#pragma unroll
    for (int w = 0; w < 8; ++w) sum += red[w * 256 + tid];
    st4_sc1(part + (size_t)(jj * 8 + kvh) * 256 + tid, __float_as_uint(sum));
    DRAIN();
  }
  __syncthreads();
  if (tid == 0) {
    const u32 old = __hip_atomic_fetch_add(cnt + (16 + jj) * 32, 1u, __ATOMIC_RELAXED, __HIP_MEMORY_SCOPE_AGENT);
    reinterpret_cast<u32*>(lds + L_FLAG)[0] = (old == 7u) ? 1u : 0u;
  }
  __syncthreads();
  if (reinterpret_cast<const u32*>(lds + L_FLAG)[0] == 0u) return;
  if (tid < 128) {
    float sv[8], gv[8];
#pragma unroll
    for (int xx = 0; xx < 8; ++xx) {
      sv[xx] = ld4_sc1(part + (size_t)(jj * 8 + xx) * 256 + tid);
      gv[xx] = ld4_sc1(part + (size_t)(jj * 8 + xx) * 256 + 128 + tid);
    }
    float ss = 0.f, gg = 0.f;
#pragma unroll
    for (int xx = 0; xx < 8; ++xx) { ss += sv[xx]; gg += gv[xx]; }
    const float sb = bfr(ss);
    const float f = bfr(bfr(gg) + bf2f(b2in));
    const float gt = bfr(f / (1.f + __expf(-f)));
    out[(tid >> 4) * DM + jj * 16 + (tid & 15)] = f2bf(bf2f(xin) + bfr(sb * gt));
  }
}

extern "C" void kernel_launch(void* const* d_in, const int* in_sizes, int n_in,
                              void* d_out, int out_size, void* d_ws, size_t ws_size,
                              hipStream_t stream) {
  const u16* x = (const u16*)d_in[0];
  const u16* kc = (const u16*)d_in[1];
  const u16* vc = (const u16*)d_in[2];
  const u16* wq = (const u16*)d_in[3];
  const u16* wo = (const u16*)d_in[4];
  const u16* w1 = (const u16*)d_in[6];
  const u16* b1 = (const u16*)d_in[7];
  const u16* w2 = (const u16*)d_in[8];
  const u16* b2 = (const u16*)d_in[9];
  unsigned char* ws = (unsigned char*)d_ws;
  k_prep<<<256, 512, 0, stream>>>(x, wq, w1, b1, ws);
  k_main<<<256, 512, 0, stream>>>(x, kc, vc, wo, w2, b2, ws, (u16*)d_out);
}
```

```cpp
#include <hip/hip_runtime.h>
#include <hip/hip_bf16.h>
#include <stdint.h>

typedef unsigned int u32;
typedef unsigned short u16;
typedef __attribute__((ext_vector_type(4))) unsigned u32x4;
typedef __attribute__((ext_vector_type(4))) float f32x4;
typedef __attribute__((ext_vector_type(2))) float f32x2;
typedef __attribute__((ext_vector_type(2))) __bf16 bf16x2;

constexpr int NB = 8, DM = 512, HQ = 32, HKV = 8, SK = 4096, DH = 128, FF = 1792;
constexpr int NSPLIT = 4;
constexpr int KPS = SK / NSPLIT;

constexpr size_t WS_CNT = 0;
constexpr size_t WS_Q = 8192;
constexpr size_t WS_H = WS_Q + NB * HQ * DH * 2;
constexpr size_t WS_PML = WS_H + 32768;
constexpr size_t WS_PO = WS_PML + NB * HQ * NSPLIT * 2 * 4;
constexpr size_t WS_PART = WS_PO + (size_t)NB * HQ * NSPLIT * DH * 4;

__device__ __forceinline__ float bf2f(u16 v) { return __uint_as_float(((u32)v) << 16); }
__device__ __forceinline__ float bflo(u32 v) { return __uint_as_float(v << 16); }
__device__ __forceinline__ float bfhi(u32 v) { return __uint_as_float(v & 0xffff0000u); }
__device__ __forceinline__ u16 f2bf(float f) { return __builtin_bit_cast(u16, (__bf16)f); }
__device__ __forceinline__ float bfr(float f) { return (float)(__bf16)f; }

__device__ __forceinline__ float dot2(u32 k, u32 q, float acc) {
  return __builtin_amdgcn_fdot2_f32_bf16(__builtin_bit_cast(bf16x2, k), __builtin_bit_cast(bf16x2, q), acc, false);
}
template <int CTRL> __device__ __forceinline__ float dpp(float x) {
  return __builtin_bit_cast(float, __builtin_amdgcn_mov_dpp(__builtin_bit_cast(int, x), CTRL, 0xf, 0xf, true));
}
constexpr int XOR1 = 0xB1, XOR2 = 0x4E, XOR7 = 0x141, XOR8 = 0x128, ROR4 = 0x124;
__device__ __forceinline__ float xrow16_max(float x) {
  auto s = __builtin_amdgcn_permlane16_swap(__float_as_uint(x), __float_as_uint(x), false, false);
  x = fmaxf(__uint_as_float(s[0]), __uint_as_float(s[1]));
  auto t = __builtin_amdgcn_permlane32_swap(__float_as_uint(x), __float_as_uint(x), false, false);
  return fmaxf(__uint_as_float(t[0]), __uint_as_float(t[1]));
}
__device__ __forceinline__ float xrow16_sum(float x) {
  auto s = __builtin_amdgcn_permlane16_swap(__float_as_uint(x), __float_as_uint(x), false, false);
  x = __uint_as_float(s[0]) + __uint_as_float(s[1]);
  auto t = __builtin_amdgcn_permlane32_swap(__float_as_uint(x), __float_as_uint(x), false, false);
  return __uint_as_float(t[0]) + __uint_as_float(t[1]);
}

__device__ __forceinline__ float wave_sum(float x) {
  x += dpp<XOR1>(x); x += dpp<XOR2>(x); x += dpp<XOR7>(x); x += dpp<XOR8>(x);
  return xrow16_sum(x);
}
__device__ __forceinline__ float sum_rg8(float x) { x += dpp<XOR8>(x); return xrow16_sum(x); }
__device__ __forceinline__ float sum_rg16(float x) { x += dpp<ROR4>(x); x += dpp<XOR8>(x); return xrow16_sum(x); }

typedef __attribute__((ext_vector_type(8))) __bf16 bf16x8;
__device__ __forceinline__ f32x4 mfma16(u32x4 a, u32x4 b, f32x4 c) {
  return __builtin_amdgcn_mfma_f32_16x16x32_bf16(__builtin_bit_cast(bf16x8, a), __builtin_bit_cast(bf16x8, b), c, 0, 0, 0);
}
__device__ __forceinline__ u32x4 ld_bfrag(const u16* p, size_t ld) {
  u32x4 r;
#pragma unroll
  for (int i = 0; i < 4; ++i) r[i] = (u32)p[(2 * i) * ld] | ((u32)p[(2 * i + 1) * ld] << 16);
  return r;
}
__device__ __forceinline__ u32 pk2(float a, float b) { return (u32)f2bf(a) | ((u32)f2bf(b) << 16); }

typedef __amdgpu_buffer_rsrc_t rsrc_t;
__device__ __forceinline__ rsrc_t mk_rsrc(const void* p, u32 bytes) { return __builtin_amdgcn_make_buffer_rsrc(const_cast<void*>(p), 0, bytes, 0x00020000); }
__device__ __forceinline__ u32x4 ld16_sc1(rsrc_t r, u32 off) { return __builtin_bit_cast(u32x4, __builtin_amdgcn_raw_buffer_load_b128(r, off, 0, 16)); }
__device__ __forceinline__ void st4_sc1(void* p, u32 v) { __hip_atomic_store(reinterpret_cast<u32*>(p), v, __ATOMIC_RELAXED, __HIP_MEMORY_SCOPE_AGENT); }
__device__ __forceinline__ void st8_sc1(void* p, float a, float b) {
  __hip_atomic_store(reinterpret_cast<unsigned long long*>(p), (unsigned long long)__float_as_uint(a) | ((unsigned long long)__float_as_uint(b) << 32), __ATOMIC_RELAXED, __HIP_MEMORY_SCOPE_AGENT);
}
__device__ __forceinline__ float ld4_sc1(const float* p) { return __hip_atomic_load(p, __ATOMIC_RELAXED, __HIP_MEMORY_SCOPE_AGENT); }
constexpr u32 SENT = 0xFFFFFFFFu;
constexpr int SPIN_CAP = 1 << 15;
#define DRAIN() asm volatile("s_waitcnt vmcnt(0)" ::: "memory")
__device__ __forceinline__ void arrive_and_wait(u32* cnt, u32 target) {
  u32 v = __hip_atomic_fetch_add(cnt, 1u, __ATOMIC_RELAXED, __HIP_MEMORY_SCOPE_AGENT) + 1u;
  for (int spin = 0; v < target && spin < (1 << 16); ++spin) {
    __builtin_amdgcn_s_sleep(1);
    v = __hip_atomic_load(cnt, __ATOMIC_RELAXED, __HIP_MEMORY_SCOPE_AGENT);
  }
}

__global__ void __launch_bounds__(512) k_prep(const u16* __restrict__ x, const u16* __restrict__ wq, const u16* __restrict__ w1,
                                              const u16* __restrict__ b1, unsigned char* __restrict__ ws) {
  __shared__ __attribute__((aligned(16))) u16 xnb[NB * DM];
  __shared__ __attribute__((aligned(16))) float red[8 * 2 * 128];
  const int tid = threadIdx.x, lane = tid & 63, wave = tid >> 6, g = lane >> 4, n = lane & 15;
  const int kvh = blockIdx.x & 7, jj = blockIdx.x >> 3;
  const u32x4 xv = *reinterpret_cast<const u32x4*>(x + wave * DM + lane * 8);
  const int jh = jj < 28 ? jj : 27;
  const int colh = kvh * 224 + jh * 8;
  const u16 b1v = b1[colh + (tid & 7)];
  __builtin_amdgcn_sched_barrier(0);
  const u16* wqp = wq + (size_t)(wave * 64 + 8 * g) * (HQ * DH) + kvh * 512 + jj * 16 + n;
  const u32x4 bq0 = ld_bfrag(wqp, HQ * DH), bq1 = ld_bfrag(wqp + (size_t)32 * (HQ * DH), HQ * DH);
  const u16* w1p = w1 + (size_t)(wave * 64 + 8 * g) * FF + colh + (n & 7);
  const u32x4 bh0 = ld_bfrag(w1p, FF), bh1 = ld_bfrag(w1p + (size_t)32 * FF, FF);
  st4_sc1(reinterpret_cast<u32*>(ws + WS_PO) + blockIdx.x * 512 + tid, SENT);
  if (tid < 256) st4_sc1(reinterpret_cast<u32*>(ws + WS_PART) + blockIdx.x * 256 + tid, SENT);
  else if (tid < 264) st4_sc1(reinterpret_cast<u32*>(ws + WS_PML) + blockIdx.x * 8 + tid - 256, SENT);
  __builtin_amdgcn_sched_barrier(0);
  {
    float f[8];
#pragma unroll
    for (int k = 0; k < 4; ++k) { f[2 * k] = bflo(xv[k]); f[2 * k + 1] = bfhi(xv[k]); }
    const float sm = ((f[0] + f[1]) + (f[2] + f[3])) + ((f[4] + f[5]) + (f[6] + f[7]));
    const float mean = wave_sum(sm) * (1.f / DM);
    float q = 0.f;
#pragma unroll
    for (int k = 0; k < 8; ++k) { const float d = f[k] - mean; q = fmaf(d, d, q); }
    const float var = bfr(wave_sum(q) * (1.f / DM));
    const float mb = bfr(mean);
    const float den = bfr(sqrtf(bfr(var + 1e-5f)));
    u32x4 o;
#pragma unroll
    for (int k = 0; k < 4; ++k) o[k] = pk2(bfr(f[2 * k] - mb) / den, bfr(f[2 * k + 1] - mb) / den);
    *reinterpret_cast<u32x4*>(xnb + wave * DM + lane * 8) = o;
  }
  __syncthreads();
  u32x4 a0 = {0u, 0u, 0u, 0u}, a1 = {0u, 0u, 0u, 0u};
  if (n < 8) { a0 = *reinterpret_cast<const u32x4*>(xnb + n * DM + wave * 64 + 8 * g); a1 = *reinterpret_cast<const u32x4*>(xnb + n * DM + wave * 64 + 32 + 8 * g); }
  f32x4 cq = {0.f, 0.f, 0.f, 0.f}, chh = {0.f, 0.f, 0.f, 0.f};
  cq = mfma16(a0, bq0, cq); chh = mfma16(a0, bh0, chh);
  cq = mfma16(a1, bq1, cq); chh = mfma16(a1, bh1, chh);
  if (g < 2) {
#pragma unroll
    for (int j = 0; j < 4; ++j) { red[(wave * 2 + 0) * 128 + (4 * g + j) * 16 + n] = cq[j]; red[(wave * 2 + 1) * 128 + (4 * g + j) * 16 + n] = chh[j]; }
  }
  __syncthreads();
  if (tid < 256) {
    const int which = tid >> 7, t = tid & 127, bb = t >> 4, nn = t & 15;
    float sum = 0.f;
#pragma unroll
    for (int w = 0; w < 8; ++w) sum += red[(w * 2 + which) * 128 + t];
    if (which == 0) {
      reinterpret_cast<u16*>(ws + WS_Q)[bb * (HQ * DH) + kvh * 512 + jj * 16 + nn] = f2bf(sum);
    } else if (nn < 8 && jj < 28) {
      const float h0 = bfr(bfr(sum) + bf2f(b1v));
      const float g0 = 0.5f * h0 * (1.f + tanhf(0.7978845608028654f * (h0 + 0.044715f * h0 * h0 * h0)));
      reinterpret_cast<u16*>(ws + WS_H)[bb * FF + colh + nn] = f2bf(g0);
    }
  }
}

constexpr int AW = 8;
constexpr int TPW = KPS / (16 * AW);
constexpr int MRG_REC = 4 + DH;
constexpr int L_OB = 0;
constexpr int L_RED = L_OB + 2048;
constexpr int L_P = L_RED + 2048;
constexpr int L_MRG = L_P + AW * 64;
constexpr int L_FLAG = L_MRG + AW * 4 * MRG_REC;
constexpr int L_TOTAL = L_FLAG + 4;

__global__ void __launch_bounds__(512) k_main(const u16* __restrict__ x, const u16* __restrict__ Kc_, const u16* __restrict__ Vc_,
                                              const u16* __restrict__ wo, const u16* __restrict__ w2, const u16* __restrict__ b2,
                                              unsigned char* __restrict__ ws, u16* __restrict__ out) {
  __shared__ __attribute__((aligned(16))) float lds[L_TOTAL];
  const int tid = threadIdx.x, lane = tid & 63, wave = __builtin_amdgcn_readfirstlane(tid >> 6), r = lane >> 4, c = lane & 15;
  const int kvh = blockIdx.x & 7, jj = blockIdx.x >> 3, b = jj >> 2, s = jj & 3;
  constexpr float SCALE = 0.08838834764831845f, LOG2E = 1.4426950408889634f;
  const u16* qg = reinterpret_cast<const u16*>(ws + WS_Q) + b * (HQ * DH) + kvh * 4 * DH + 8 * c;
  u32x4 qpk[4];
#pragma unroll
  for (int g = 0; g < 4; ++g) qpk[g] = *reinterpret_cast<const u32x4*>(qg + g * DH);
  __builtin_amdgcn_sched_barrier(0);
  const size_t base = ((size_t)(b * HKV + kvh) * SK + (size_t)s * KPS + wave * 16 + r) * DH + 8 * c;
  const u16* kb = Kc_ + base;
  const u16* vb = Vc_ + base;
  u32x4 Kr[3][4], Vr[3][4];
#define ISSUE(i_) do { _Pragma("unroll") for (int t_ = 0; t_ < 4; ++t_) { \
    Kr[(i_) % 3][t_] = __builtin_nontemporal_load(reinterpret_cast<const u32x4*>(kb + (size_t)((i_) * AW * 16 + t_ * 4) * DH)); \
    Vr[(i_) % 3][t_] = __builtin_nontemporal_load(reinterpret_cast<const u32x4*>(vb + (size_t)((i_) * AW * 16 + t_ * 4) * DH)); } } while (0)
  ISSUE(0);
  ISSUE(1);
  __builtin_amdgcn_sched_barrier(0);
  const int fo = tid & 127;
  const u16 xin = x[(fo >> 4) * DM + jj * 16 + (fo & 15)], b2in = b2[jj * 16 + (fo & 15)];
  const u16* wop = wo + (size_t)(kvh * 512 + wave * 64 + 8 * r) * DM + jj * 16 + c;
  const u32x4 bo0 = ld_bfrag(wop, DM), bo1 = ld_bfrag(wop + 32 * DM, DM);
  const int w7 = wave < 7 ? wave : 6;
  u32x4 b2f = ld_bfrag(w2 + (size_t)(kvh * 224 + w7 * 32 + 8 * r) * DM + jj * 16 + c, DM);
  u32x4 ahf = *reinterpret_cast<const u32x4*>(reinterpret_cast<const u16*>(ws + WS_H) + (c & 7) * FF + kvh * 224 + w7 * 32 + 8 * r);
  __builtin_amdgcn_sched_barrier(0);
  float m = -INFINITY, l = 0.f, acc[32];
#pragma unroll
  for (int k = 0; k < 32; ++k) acc[k] = 0.f;
#pragma unroll
  for (int i = 0; i < TPW; ++i) {
    if (i + 2 < TPW) ISSUE(i + 2);
    float xs[16];
#pragma unroll
    for (int t = 0; t < 4; ++t)
#pragma unroll
      for (int g = 0; g < 4; ++g) {
        float d = 0.f;
#pragma unroll
        for (int k = 0; k < 4; ++k) d = dot2(Kr[i % 3][t][k], qpk[g][k], d);
        xs[t * 4 + g] = d;
      }
#define TR_STEP(HALF, CTRL, BIT) _Pragma("unroll") for (int h = 0; h < (HALF); ++h) { \
      const float lo_ = xs[h], hi_ = xs[h + (HALF)]; const float t1_ = lo_ + dpp<CTRL>(lo_), t2_ = hi_ + dpp<CTRL>(hi_); \
      xs[h] = (c & (BIT)) ? t2_ : t1_; }
    TR_STEP(8, XOR8, 8) TR_STEP(4, XOR7, 4) TR_STEP(2, XOR2, 2) TR_STEP(1, XOR1, 1)
#undef TR_STEP
    const float sc = bfr(bfr(xs[0]) * SCALE) * LOG2E;
    float pm = sc; pm = fmaxf(pm, dpp<ROR4>(pm)); pm = fmaxf(pm, dpp<XOR8>(pm)); pm = xrow16_max(pm);
    const float mn = fmaxf(m, pm);
    const float alpha = __builtin_amdgcn_exp2f(m - mn), p = __builtin_amdgcn_exp2f(sc - mn);
    l = fmaf(l, alpha, p); m = mn;
    {
      const float a0 = dpp<0x00>(alpha), a1 = dpp<0x55>(alpha), a2 = dpp<0xAA>(alpha), a3 = dpp<0xFF>(alpha);
#pragma unroll
      for (int k = 0; k < 8; ++k) { acc[k] *= a0; acc[8 + k] *= a1; acc[16 + k] *= a2; acc[24 + k] *= a3; }
    }
    float* prow = lds + L_P + (wave * 4 + r) * 16;
    prow[c] = p;
    asm volatile("s_waitcnt lgkmcnt(0)" ::: "memory");
    f32x4 pr[4];
#pragma unroll
    for (int u = 0; u < 4; ++u) pr[u] = *reinterpret_cast<const f32x4*>(prow + 4 * u);
#pragma unroll
    for (int t = 0; t < 4; ++t) {
      float vf[8];
#pragma unroll
      for (int k = 0; k < 4; ++k) { vf[2 * k] = bflo(Vr[i % 3][t][k]); vf[2 * k + 1] = bfhi(Vr[i % 3][t][k]); }
#pragma unroll
      for (int g = 0; g < 4; ++g) {
#pragma unroll
        for (int k = 0; k < 8; ++k) acc[g * 8 + k] = fmaf(pr[t][g], vf[k], acc[g * 8 + k]);
      }
    }
  }
#undef ISSUE
#pragma unroll
  for (int k = 0; k < 32; ++k) acc[k] = xrow16_sum(acc[k]);
  { float t = l; t += dpp<ROR4>(t); t += dpp<XOR8>(t); l = xrow16_sum(t); }
  float* mrg = lds + L_MRG;
  if (r == 0) {
#pragma unroll
    for (int g = 0; g < 4; ++g) {
      float* rec = mrg + (wave * 4 + g) * MRG_REC;
#pragma unroll
      for (int k = 0; k < 8; k += 4) *reinterpret_cast<f32x4*>(rec + 4 + 8 * c + k) = f32x4{acc[g * 8 + k], acc[g * 8 + k + 1], acc[g * 8 + k + 2], acc[g * 8 + k + 3]};
      if (c == g) { rec[0] = m; rec[1] = l; }
    }
  }
  __syncthreads();
  if (wave < 4) {
    const int g = wave, d0 = 2 * lane;
    float M = -INFINITY;
#pragma unroll
    for (int w = 0; w < AW; ++w) M = fmaxf(M, mrg[(w * 4 + g) * MRG_REC]);
    float L = 0.f, o0 = 0.f, o1 = 0.f;
#pragma unroll
    for (int w = 0; w < AW; ++w) {
      const float* rec = mrg + (w * 4 + g) * MRG_REC;
      const float wt = __builtin_amdgcn_exp2f(rec[0] - M);
      L = fmaf(wt, rec[1], L); o0 = fmaf(wt, rec[4 + d0], o0); o1 = fmaf(wt, rec[4 + d0 + 1], o1);
    }
    const size_t slot = (size_t)(b * HQ + kvh * 4 + g) * NSPLIT + s;
    st8_sc1(reinterpret_cast<float*>(ws + WS_PO) + slot * DH + d0, o0, o1);
    if (lane == 0) st8_sc1(reinterpret_cast<float*>(ws + WS_PML) + slot * 2, M, L);
  }
  u16* ob = reinterpret_cast<u16*>(lds + L_OB);
  float* red = lds + L_RED;
  {
    const int d0 = c * 8;
    const u32 slot = (u32)(wave * HQ + kvh * 4 + r) * NSPLIT;
    const rsrc_t rp = mk_rsrc(ws + WS_PML, (u32)(WS_PART - WS_PML));
    u32x4 ml[NSPLIT / 2], a0[NSPLIT], a1[NSPLIT];
    u32 offm = slot * 8, offa = (u32)(WS_PO - WS_PML) + (slot * DH + d0) * 4;
    for (int spin = 0; spin < SPIN_CAP; ++spin) {
      bool ok = true;
      asm volatile("" : "+v"(offm));
#pragma unroll
      for (int ss = 0; ss < NSPLIT / 2; ++ss) { ml[ss] = ld16_sc1(rp, offm + 16 * ss); ok = ok && ml[ss][0] != SENT && ml[ss][1] != SENT && ml[ss][2] != SENT && ml[ss][3] != SENT; }
      if (__all(ok)) break;
      __builtin_amdgcn_s_sleep(4);
      asm volatile("" ::: "memory");
    }
    for (int spin = 0; spin < SPIN_CAP; ++spin) {
      bool ok = true;
      asm volatile("" : "+v"(offa));
#pragma unroll
      for (int ss = 0; ss < NSPLIT; ++ss) {
        a0[ss] = ld16_sc1(rp, offa + ss * DH * 4);
        a1[ss] = ld16_sc1(rp, offa + ss * DH * 4 + 16);
      }
#pragma unroll
      for (int ss = 0; ss < NSPLIT; ++ss)
#pragma unroll
        for (int k = 0; k < 4; ++k) ok = ok && a0[ss][k] != SENT && a1[ss][k] != SENT;
      if (__all(ok)) break;
      __builtin_amdgcn_s_sleep(2);
      asm volatile("" ::: "memory");
    }
    float M = -INFINITY;
#pragma unroll
    for (int ss = 0; ss < NSPLIT; ++ss) M = fmaxf(M, __uint_as_float(ml[ss >> 1][(ss & 1) * 2]));
    float L = 0.f; f32x4 o0 = {0.f, 0.f, 0.f, 0.f}, o1 = {0.f, 0.f, 0.f, 0.f};
#pragma unroll
    for (int ss = 0; ss < NSPLIT; ++ss) {
      const float w = __builtin_amdgcn_exp2f(__uint_as_float(ml[ss >> 1][(ss & 1) * 2]) - M);
      L = fmaf(w, __uint_as_float(ml[ss >> 1][(ss & 1) * 2 + 1]), L);
      o0 += w * __builtin_bit_cast(f32x4, a0[ss]); o1 += w * __builtin_bit_cast(f32x4, a1[ss]);
    }
    const float inv = 1.f / L;
    *reinterpret_cast<u32x4*>(ob + wave * 512 + r * DH + d0) = u32x4{pk2(o0[0] * inv, o0[1] * inv), pk2(o0[2] * inv, o0[3] * inv), pk2(o1[0] * inv, o1[1] * inv), pk2(o1[2] * inv, o1[3] * inv)};
  }
  __syncthreads();
  {
    u32x4 a0 = {0u, 0u, 0u, 0u}, a1 = {0u, 0u, 0u, 0u};
    if (c < 8) { a0 = *reinterpret_cast<const u32x4*>(ob + c * 512 + wave * 64 + 8 * r); a1 = *reinterpret_cast<const u32x4*>(ob + c * 512 + wave * 64 + 32 + 8 * r); }
    if (c >= 8 || wave == 7) ahf = u32x4{0u, 0u, 0u, 0u};
    f32x4 co = {0.f, 0.f, 0.f, 0.f}, cg = {0.f, 0.f, 0.f, 0.f};
    co = mfma16(a0, bo0, co); cg = mfma16(ahf, b2f, cg); co = mfma16(a1, bo1, co);
    if (r < 2) {
#pragma unroll
      for (int j = 0; j < 4; ++j) { red[(wave * 2 + 0) * 128 + (4 * r + j) * 16 + c] = co[j]; red[(wave * 2 + 1) * 128 + (4 * r + j) * 16 + c] = cg[j]; }
    }
  }
  __syncthreads();
  float* part = reinterpret_cast<float*>(ws + WS_PART);
  if (tid < 256) {
    float sum = 0.f;
#pragma unroll
    for (int w = 0; w < 8; ++w) sum += red[w * 256 + tid];
    if (kvh != 7) { st4_sc1(part + (size_t)(jj * 8 + kvh) * 256 + tid, __float_as_uint(sum)); return; }
    u32 pv[7];
    for (int spin = 0; spin < SPIN_CAP; ++spin) {
      bool ok = true;
#pragma unroll
      for (int xx = 0; xx < 7; ++xx) { pv[xx] = __float_as_uint(ld4_sc1(part + (size_t)(jj * 8 + xx) * 256 + tid)); ok = ok && pv[xx] != SENT; }
      if (__all(ok)) break;
      __builtin_amdgcn_s_sleep(1);
    }
    float tot = 0.f;
#pragma unroll
    for (int xx = 0; xx < 7; ++xx) tot += __uint_as_float(pv[xx]);
    tot += sum;
    lds[L_P + tid] = tot;
  } else {
    if (kvh != 7) return;
  }
  __syncthreads();
  if (tid < 128) {
    const float sb = bfr(lds[L_P + tid]);
    const float f = bfr(bfr(lds[L_P + 128 + tid]) + bf2f(b2in));
    const float gt = bfr(f / (1.f + __expf(-f)));
    out[(tid >> 4) * DM + jj * 16 + (tid & 15)] = f2bf(bf2f(xin) + bfr(sb * gt));
  }
}

extern "C" void kernel_launch(void* const* d_in, const int* in_sizes, int n_in,
                              void* d_out, int out_size, void* d_ws, size_t ws_size,
                              hipStream_t stream) {
  const u16* x = (const u16*)d_in[0];
  const u16* kc = (const u16*)d_in[1];
  const u16* vc = (const u16*)d_in[2];
  const u16* wq = (const u16*)d_in[3];
  const u16* wo = (const u16*)d_in[4];
  const u16* w1 = (const u16*)d_in[6];
  const u16* b1 = (const u16*)d_in[7];
  const u16* w2 = (const u16*)d_in[8];
  const u16* b2 = (const u16*)d_in[9];
  unsigned char* ws = (unsigned char*)d_ws;
  k_prep<<<256, 512, 0, stream>>>(x, wq, w1, b1, ws);
  k_main<<<256, 512, 0, stream>>>(x, kc, vc, wo, w2, b2, ws, (u16*)d_out);
}
```

```cpp
#include <hip/hip_runtime.h>
#include <hip/hip_bf16.h>
#include <stdint.h>

typedef unsigned int u32;
typedef unsigned short u16;
typedef __attribute__((ext_vector_type(4))) unsigned u32x4;
typedef __attribute__((ext_vector_type(4))) float f32x4;
typedef __attribute__((ext_vector_type(2))) float f32x2;
typedef __attribute__((ext_vector_type(2))) __bf16 bf16x2;

constexpr int NB = 8, DM = 512, HQ = 32, HKV = 8, SK = 4096, DH = 128, FF = 1792;
constexpr int NSPLIT = 4;
constexpr int KPS = SK / NSPLIT;

constexpr size_t WS_Q = 0;
constexpr size_t WS_H = WS_Q + NB * HQ * DH * 2;
constexpr size_t WS_PML = WS_H + 32768;
constexpr size_t WS_PO = WS_PML + NB * HQ * NSPLIT * 2 * 4;
constexpr size_t WS_PART = WS_PO + (size_t)NB * HQ * NSPLIT * DH * 4;

__device__ __forceinline__ float bf2f(u16 v) { return __uint_as_float(((u32)v) << 16); }
__device__ __forceinline__ float bflo(u32 v) { return __uint_as_float(v << 16); }
__device__ __forceinline__ float bfhi(u32 v) { return __uint_as_float(v & 0xffff0000u); }
__device__ __forceinline__ u16 f2bf(float f) { return __builtin_bit_cast(u16, (__bf16)f); }
__device__ __forceinline__ float bfr(float f) { return (float)(__bf16)f; }

__device__ __forceinline__ float dot2(u32 k, u32 q, float acc) {
  return __builtin_amdgcn_fdot2_f32_bf16(__builtin_bit_cast(bf16x2, k), __builtin_bit_cast(bf16x2, q), acc, false);
}
template <int CTRL> __device__ __forceinline__ float dpp(float x) {
  return __builtin_bit_cast(float, __builtin_amdgcn_mov_dpp(__builtin_bit_cast(int, x), CTRL, 0xf, 0xf, true));
}
constexpr int XOR1 = 0xB1, XOR2 = 0x4E, XOR7 = 0x141, XOR8 = 0x128, ROR4 = 0x124;
__device__ __forceinline__ float xrow16_max(float x) {
  auto s = __builtin_amdgcn_permlane16_swap(__float_as_uint(x), __float_as_uint(x), false, false);
  x = fmaxf(__uint_as_float(s[0]), __uint_as_float(s[1]));
  auto t = __builtin_amdgcn_permlane32_swap(__float_as_uint(x), __float_as_uint(x), false, false);
  return fmaxf(__uint_as_float(t[0]), __uint_as_float(t[1]));
}
__device__ __forceinline__ float xrow16_sum(float x) {
  auto s = __builtin_amdgcn_permlane16_swap(__float_as_uint(x), __float_as_uint(x), false, false);
  x = __uint_as_float(s[0]) + __uint_as_float(s[1]);
  auto t = __builtin_amdgcn_permlane32_swap(__float_as_uint(x), __float_as_uint(x), false, false);
  return __uint_as_float(t[0]) + __uint_as_float(t[1]);
}

__device__ __forceinline__ float wave_sum(float x) {
  x += dpp<XOR1>(x); x += dpp<XOR2>(x); x += dpp<XOR7>(x); x += dpp<XOR8>(x);
  return xrow16_sum(x);
}

typedef __attribute__((ext_vector_type(8))) __bf16 bf16x8;
__device__ __forceinline__ f32x4 mfma16(u32x4 a, u32x4 b, f32x4 c) {
  return __builtin_amdgcn_mfma_f32_16x16x32_bf16(__builtin_bit_cast(bf16x8, a), __builtin_bit_cast(bf16x8, b), c, 0, 0, 0);
}
__device__ __forceinline__ u32x4 ld_bfrag(const u16* p, size_t ld) {
  u32x4 r;
#pragma unroll
  for (int i = 0; i < 4; ++i) r[i] = (u32)p[(2 * i) * ld] | ((u32)p[(2 * i + 1) * ld] << 16);
  return r;
}
__device__ __forceinline__ u32 pk2(float a, float b) { return (u32)f2bf(a) | ((u32)f2bf(b) << 16); }

typedef __amdgpu_buffer_rsrc_t rsrc_t;
__device__ __forceinline__ rsrc_t mk_rsrc(const void* p, u32 bytes) { return __builtin_amdgcn_make_buffer_rsrc(const_cast<void*>(p), 0, bytes, 0x00020000); }
__device__ __forceinline__ u32x4 ld16_sc1(rsrc_t r, u32 off) { return __builtin_bit_cast(u32x4, __builtin_amdgcn_raw_buffer_load_b128(r, off, 0, 16)); }
__device__ __forceinline__ void st4_sc1(void* p, u32 v) { __hip_atomic_store(reinterpret_cast<u32*>(p), v, __ATOMIC_RELAXED, __HIP_MEMORY_SCOPE_AGENT); }
__device__ __forceinline__ void st8_sc1(void* p, float a, float b) {
  __hip_atomic_store(reinterpret_cast<unsigned long long*>(p), (unsigned long long)__float_as_uint(a) | ((unsigned long long)__float_as_uint(b) << 32), __ATOMIC_RELAXED, __HIP_MEMORY_SCOPE_AGENT);
}
__device__ __forceinline__ float ld4_sc1(const float* p) { return __hip_atomic_load(p, __ATOMIC_RELAXED, __HIP_MEMORY_SCOPE_AGENT); }
constexpr u32 SENT = 0xFFFFFFFFu;
constexpr int SPIN_CAP = 1 << 15;

__global__ void __launch_bounds__(512) k_prep(const u16* __restrict__ x, const u16* __restrict__ wq, const u16* __restrict__ w1,
                                              const u16* __restrict__ b1, unsigned char* __restrict__ ws) {
  __shared__ __attribute__((aligned(16))) u16 xnb[NB * DM];
  __shared__ __attribute__((aligned(16))) float red[8 * 2 * 128];
  const int tid = threadIdx.x, lane = tid & 63, wave = tid >> 6, g = lane >> 4, n = lane & 15;
  const int kvh = blockIdx.x & 7, jj = blockIdx.x >> 3;
  const u32x4 xv = *reinterpret_cast<const u32x4*>(x + wave * DM + lane * 8);
  const int jh = jj < 28 ? jj : 27;
  const int colh = kvh * 224 + jh * 8;
  const u16 b1v = b1[colh + (tid & 7)];
  __builtin_amdgcn_sched_barrier(0);
  const u16* wqp = wq + (size_t)(wave * 64 + 8 * g) * (HQ * DH) + kvh * 512 + jj * 16 + n;
  const u32x4 bq0 = ld_bfrag(wqp, HQ * DH), bq1 = ld_bfrag(wqp + (size_t)32 * (HQ * DH), HQ * DH);
  const u16* w1p = w1 + (size_t)(wave * 64 + 8 * g) * FF + colh + (n & 7);
  const u32x4 bh0 = ld_bfrag(w1p, FF), bh1 = ld_bfrag(w1p + (size_t)32 * FF, FF);
  st4_sc1(reinterpret_cast<u32*>(ws + WS_PO) + blockIdx.x * 512 + tid, SENT);
  if (tid < 256) st4_sc1(reinterpret_cast<u32*>(ws + WS_PART) + blockIdx.x * 256 + tid, SENT);
  else if (tid < 264) st4_sc1(reinterpret_cast<u32*>(ws + WS_PML) + blockIdx.x * 8 + tid - 256, SENT);
  __builtin_amdgcn_sched_barrier(0);
  {
    float f[8];
#pragma unroll
    for (int k = 0; k < 4; ++k) { f[2 * k] = bflo(xv[k]); f[2 * k + 1] = bfhi(xv[k]); }
    const float sm = ((f[0] + f[1]) + (f[2] + f[3])) + ((f[4] + f[5]) + (f[6] + f[7]));
    const float mean = wave_sum(sm) * (1.f / DM);
    float q = 0.f;
#pragma unroll
    for (int k = 0; k < 8; ++k) { const float d = f[k] - mean; q = fmaf(d, d, q); }
    const float var = bfr(wave_sum(q) * (1.f / DM));
    const float mb = bfr(mean);
    const float den = bfr(sqrtf(bfr(var + 1e-5f)));
    u32x4 o;
#pragma unroll
    for (int k = 0; k < 4; ++k) o[k] = pk2(bfr(f[2 * k] - mb) / den, bfr(f[2 * k + 1] - mb) / den);
    *reinterpret_cast<u32x4*>(xnb + wave * DM + lane * 8) = o;
  }
  __syncthreads();
  u32x4 a0 = {0u, 0u, 0u, 0u}, a1 = {0u, 0u, 0u, 0u};
  if (n < 8) { a0 = *reinterpret_cast<const u32x4*>(xnb + n * DM + wave * 64 + 8 * g); a1 = *reinterpret_cast<const u32x4*>(xnb + n * DM + wave * 64 + 32 + 8 * g); }
  f32x4 cq = {0.f, 0.f, 0.f, 0.f}, chh = {0.f, 0.f, 0.f, 0.f};
  cq = mfma16(a0, bq0, cq); chh = mfma16(a0, bh0, chh);
  cq = mfma16(a1, bq1, cq); chh = mfma16(a1, bh1, chh);
  if (g < 2) {
#pragma unroll
    for (int j = 0; j < 4; ++j) { red[(wave * 2 + 0) * 128 + (4 * g + j) * 16 + n] = cq[j]; red[(wave * 2 + 1) * 128 + (4 * g + j) * 16 + n] = chh[j]; }
  }
  __syncthreads();
  if (tid < 256) {
    const int which = tid >> 7, t = tid & 127, bb = t >> 4, nn = t & 15;
    float sum = 0.f;
#pragma unroll
    for (int w = 0; w < 8; ++w) sum += red[(w * 2 + which) * 128 + t];
    if (which == 0) {
      reinterpret_cast<u16*>(ws + WS_Q)[bb * (HQ * DH) + kvh * 512 + jj * 16 + nn] = f2bf(sum);
    } else if (nn < 8 && jj < 28) {
      const float h0 = bfr(bfr(sum) + bf2f(b1v));
      const float g0 = 0.5f * h0 * (1.f + tanhf(0.7978845608028654f * (h0 + 0.044715f * h0 * h0 * h0)));
      reinterpret_cast<u16*>(ws + WS_H)[bb * FF + colh + nn] = f2bf(g0);
    }
  }
}

constexpr int AW = 8;
constexpr int TPW = KPS / (16 * AW);
constexpr int MRG_REC = 4 + DH;
constexpr int L_OB = 0;
constexpr int L_RED = L_OB + 2048;
constexpr int L_P = L_RED + 2048;
constexpr int L_MRG = L_P + AW * 64;
constexpr int L_TOTAL = L_MRG + AW * 4 * MRG_REC;

__global__ void __launch_bounds__(512) k_main(const u16* __restrict__ x, const u16* __restrict__ Kc_, const u16* __restrict__ Vc_,
                                              const u16* __restrict__ wo, const u16* __restrict__ w2, const u16* __restrict__ b2,
                                              unsigned char* __restrict__ ws, u16* __restrict__ out) {
  __shared__ __attribute__((aligned(16))) float lds[L_TOTAL];
  const int tid = threadIdx.x, lane = tid & 63, wave = __builtin_amdgcn_readfirstlane(tid >> 6), r = lane >> 4, c = lane & 15;
  const int kvh = blockIdx.x & 7, jj = blockIdx.x >> 3, b = jj >> 2, s = jj & 3;
  constexpr float SCALE = 0.08838834764831845f, LOG2E = 1.4426950408889634f;
  const u16* qg = reinterpret_cast<const u16*>(ws + WS_Q) + b * (HQ * DH) + kvh * 4 * DH + 8 * c;
  u32x4 qpk[4];
#pragma unroll
  for (int g = 0; g < 4; ++g) qpk[g] = *reinterpret_cast<const u32x4*>(qg + g * DH);
  __builtin_amdgcn_sched_barrier(0);
  const size_t base = ((size_t)(b * HKV + kvh) * SK + (size_t)s * KPS + wave * 16 + r) * DH + 8 * c;
  const u16* kb = Kc_ + base;
  const u16* vb = Vc_ + base;
  u32x4 Kr[3][4], Vr[3][4];
#define ISSUE(i_) do { _Pragma("unroll") for (int t_ = 0; t_ < 4; ++t_) { \
    Kr[(i_) % 3][t_] = __builtin_nontemporal_load(reinterpret_cast<const u32x4*>(kb + (size_t)((i_) * AW * 16 + t_ * 4) * DH)); \
    Vr[(i_) % 3][t_] = __builtin_nontemporal_load(reinterpret_cast<const u32x4*>(vb + (size_t)((i_) * AW * 16 + t_ * 4) * DH)); } } while (0)
  ISSUE(0);
  ISSUE(1);
  __builtin_amdgcn_sched_barrier(0);
  const int fo = tid & 127;
  const u16 xin = x[(fo >> 4) * DM + jj * 16 + (fo & 15)], b2in = b2[jj * 16 + (fo & 15)];
  const u16* wop = wo + (size_t)(kvh * 512 + wave * 64 + 8 * r) * DM + jj * 16 + c;
  const u32x4 bo0 = ld_bfrag(wop, DM), bo1 = ld_bfrag(wop + 32 * DM, DM);
  const int w7 = wave < 7 ? wave : 6;
  u32x4 b2f = ld_bfrag(w2 + (size_t)(kvh * 224 + w7 * 32 + 8 * r) * DM + jj * 16 + c, DM);
  u32x4 ahf = *reinterpret_cast<const u32x4*>(reinterpret_cast<const u16*>(ws + WS_H) + (c & 7) * FF + kvh * 224 + w7 * 32 + 8 * r);
  __builtin_amdgcn_sched_barrier(0);
  float m = -INFINITY, l = 0.f;
  f32x2 acc[16];
#pragma unroll
  for (int k = 0; k < 16; ++k) acc[k] = f32x2{0.f, 0.f};
#pragma unroll
  for (int i = 0; i < TPW; ++i) {
    if (i + 2 < TPW) ISSUE(i + 2);
    float xs[16];
#pragma unroll
    for (int t = 0; t < 4; ++t)
#pragma unroll
      for (int g = 0; g < 4; ++g) {
        float d = 0.f;
#pragma unroll
        for (int k = 0; k < 4; ++k) d = dot2(Kr[i % 3][t][k], qpk[g][k], d);
        xs[t * 4 + g] = d;
      }
#define TR_STEP(HALF, CTRL, BIT) _Pragma("unroll") for (int h = 0; h < (HALF); ++h) { \
      const float lo_ = xs[h], hi_ = xs[h + (HALF)]; const float t1_ = lo_ + dpp<CTRL>(lo_), t2_ = hi_ + dpp<CTRL>(hi_); \
      xs[h] = (c & (BIT)) ? t2_ : t1_; }
    TR_STEP(8, XOR8, 8) TR_STEP(4, XOR7, 4) TR_STEP(2, XOR2, 2) TR_STEP(1, XOR1, 1)
#undef TR_STEP
    const float sc = bfr(bfr(xs[0]) * SCALE) * LOG2E;
    float pm = sc; pm = fmaxf(pm, dpp<ROR4>(pm)); pm = fmaxf(pm, dpp<XOR8>(pm)); pm = xrow16_max(pm);
    const float mn = fmaxf(m, pm);
    const float alpha = __builtin_amdgcn_exp2f(m - mn), p = __builtin_amdgcn_exp2f(sc - mn);
    l = fmaf(l, alpha, p); m = mn;
    {
      const float a0 = dpp<0x00>(alpha), a1 = dpp<0x55>(alpha), a2 = dpp<0xAA>(alpha), a3 = dpp<0xFF>(alpha);
#pragma unroll
      for (int k = 0; k < 4; ++k) { acc[k] *= f32x2{a0, a0}; acc[4 + k] *= f32x2{a1, a1}; acc[8 + k] *= f32x2{a2, a2}; acc[12 + k] *= f32x2{a3, a3}; }
    }
    float* prow = lds + L_P + (wave * 4 + r) * 16;
    prow[c] = p;
    asm volatile("s_waitcnt lgkmcnt(0)" ::: "memory");
    f32x4 pr[4];
#pragma unroll
    for (int u = 0; u < 4; ++u) pr[u] = *reinterpret_cast<const f32x4*>(prow + 4 * u);
#pragma unroll
    for (int t = 0; t < 4; ++t) {
      f32x2 vf[4];
#pragma unroll
      for (int k = 0; k < 4; ++k) vf[k] = f32x2{bflo(Vr[i % 3][t][k]), bfhi(Vr[i % 3][t][k])};
#pragma unroll
      for (int g = 0; g < 4; ++g) {
        const f32x2 pp = {pr[t][g], pr[t][g]};
#pragma unroll
        for (int k = 0; k < 4; ++k) acc[g * 4 + k] = __builtin_elementwise_fma(pp, vf[k], acc[g * 4 + k]);
      }
    }
  }
#undef ISSUE
  float fin[8];
  {
    float h1[16];
#pragma unroll
    for (int k = 0; k < 8; ++k) {
      const float x0 = acc[k >> 1][k & 1], x1 = acc[4 + (k >> 1)][k & 1], x2 = acc[8 + (k >> 1)][k & 1], x3 = acc[12 + (k >> 1)][k & 1];
      auto s01 = __builtin_amdgcn_permlane16_swap(__float_as_uint(x0), __float_as_uint(x1), false, false);
      auto s23 = __builtin_amdgcn_permlane16_swap(__float_as_uint(x2), __float_as_uint(x3), false, false);
      h1[k] = __uint_as_float(s01[0]) + __uint_as_float(s01[1]);
      h1[8 + k] = __uint_as_float(s23[0]) + __uint_as_float(s23[1]);
    }
#pragma unroll
    for (int k = 0; k < 8; ++k) {
      auto t = __builtin_amdgcn_permlane32_swap(__float_as_uint(h1[k]), __float_as_uint(h1[8 + k]), false, false);
      fin[k] = __uint_as_float(t[0]) + __uint_as_float(t[1]);
    }
  }
  { float t = l; t += dpp<ROR4>(t); t += dpp<XOR8>(t); l = xrow16_sum(t); }
  float* mrg = lds + L_MRG;
  {
    float* rec = mrg + (wave * 4 + r) * MRG_REC;
    *reinterpret_cast<f32x4*>(rec + 4 + 8 * c) = f32x4{fin[0], fin[1], fin[2], fin[3]};
    *reinterpret_cast<f32x4*>(rec + 4 + 8 * c + 4) = f32x4{fin[4], fin[5], fin[6], fin[7]};
    if (lane < 4) { float* rc = mrg + (wave * 4 + lane) * MRG_REC; rc[0] = m; rc[1] = l; }
  }
  __syncthreads();
  if (wave < 4) {
    const int g = wave, d0 = 2 * lane;
    float M = -INFINITY;
#pragma unroll
    for (int w = 0; w < AW; ++w) M = fmaxf(M, mrg[(w * 4 + g) * MRG_REC]);
    float L = 0.f, o0 = 0.f, o1 = 0.f;
#pragma unroll
    for (int w = 0; w < AW; ++w) {
      const float* rec = mrg + (w * 4 + g) * MRG_REC;
      const float wt = __builtin_amdgcn_exp2f(rec[0] - M);
      L = fmaf(wt, rec[1], L); o0 = fmaf(wt, rec[4 + d0], o0); o1 = fmaf(wt, rec[4 + d0 + 1], o1);
    }
    const size_t slot = (size_t)(b * HQ + kvh * 4 + g) * NSPLIT + s;
    st8_sc1(reinterpret_cast<float*>(ws + WS_PO) + slot * DH + d0, o0, o1);
    if (lane == 0) st8_sc1(reinterpret_cast<float*>(ws + WS_PML) + slot * 2, M, L);
  }
  u16* ob = reinterpret_cast<u16*>(lds + L_OB);
  float* red = lds + L_RED;
  {
    const int d0 = c * 8;
    const u32 slot = (u32)(wave * HQ + kvh * 4 + r) * NSPLIT;
    const rsrc_t rp = mk_rsrc(ws + WS_PML, (u32)(WS_PART - WS_PML));
    u32x4 ml[NSPLIT / 2], a0[NSPLIT], a1[NSPLIT];
    u32 offm = slot * 8, offa = (u32)(WS_PO - WS_PML) + (slot * DH + d0) * 4;
    for (int spin = 0; spin < SPIN_CAP; ++spin) {
      bool ok = true;
      asm volatile("" : "+v"(offm));
#pragma unroll
      for (int ss = 0; ss < NSPLIT / 2; ++ss) { ml[ss] = ld16_sc1(rp, offm + 16 * ss); ok = ok && ml[ss][0] != SENT && ml[ss][1] != SENT && ml[ss][2] != SENT && ml[ss][3] != SENT; }
      if (__all(ok)) break;
      __builtin_amdgcn_s_sleep(4);
      asm volatile("" ::: "memory");
    }
    for (int spin = 0; spin < SPIN_CAP; ++spin) {
      bool ok = true;
      asm volatile("" : "+v"(offa));
#pragma unroll
      for (int ss = 0; ss < NSPLIT; ++ss) {
        a0[ss] = ld16_sc1(rp, offa + ss * DH * 4);
        a1[ss] = ld16_sc1(rp, offa + ss * DH * 4 + 16);
      }
#pragma unroll
      for (int ss = 0; ss < NSPLIT; ++ss)
#pragma unroll
        for (int k = 0; k < 4; ++k) ok = ok && a0[ss][k] != SENT && a1[ss][k] != SENT;
      if (__all(ok)) break;
      __builtin_amdgcn_s_sleep(2);
      asm volatile("" ::: "memory");
    }
    float M = -INFINITY;
#pragma unroll
    for (int ss = 0; ss < NSPLIT; ++ss) M = fmaxf(M, __uint_as_float(ml[ss >> 1][(ss & 1) * 2]));
    float L = 0.f; f32x4 o0 = {0.f, 0.f, 0.f, 0.f}, o1 = {0.f, 0.f, 0.f, 0.f};
#pragma unroll
    for (int ss = 0; ss < NSPLIT; ++ss) {
      const float w = __builtin_amdgcn_exp2f(__uint_as_float(ml[ss >> 1][(ss & 1) * 2]) - M);
      L = fmaf(w, __uint_as_float(ml[ss >> 1][(ss & 1) * 2 + 1]), L);
      o0 += w * __builtin_bit_cast(f32x4, a0[ss]); o1 += w * __builtin_bit_cast(f32x4, a1[ss]);
    }
    const float inv = 1.f / L;
    *reinterpret_cast<u32x4*>(ob + wave * 512 + r * DH + d0) = u32x4{pk2(o0[0] * inv, o0[1] * inv), pk2(o0[2] * inv, o0[3] * inv), pk2(o1[0] * inv, o1[1] * inv), pk2(o1[2] * inv, o1[3] * inv)};
  }
  __syncthreads();
  {
    u32x4 a0 = {0u, 0u, 0u, 0u}, a1 = {0u, 0u, 0u, 0u};
    if (c < 8) { a0 = *reinterpret_cast<const u32x4*>(ob + c * 512 + wave * 64 + 8 * r); a1 = *reinterpret_cast<const u32x4*>(ob + c * 512 + wave * 64 + 32 + 8 * r); }
    if (c >= 8 || wave == 7) ahf = u32x4{0u, 0u, 0u, 0u};
    f32x4 co = {0.f, 0.f, 0.f, 0.f}, cg = {0.f, 0.f, 0.f, 0.f};
    co = mfma16(a0, bo0, co); cg = mfma16(ahf, b2f, cg); co = mfma16(a1, bo1, co);
    if (r < 2) {
#pragma unroll
      for (int j = 0; j < 4; ++j) { red[(wave * 2 + 0) * 128 + (4 * r + j) * 16 + c] = co[j]; red[(wave * 2 + 1) * 128 + (4 * r + j) * 16 + c] = cg[j]; }
    }
  }
  __syncthreads();
  float* part = reinterpret_cast<float*>(ws + WS_PART);
  if (tid < 256) {
    float sum = 0.f;
#pragma unroll
    for (int w = 0; w < 8; ++w) sum += red[w * 256 + tid];
    if (kvh != 7) { st4_sc1(part + (size_t)(jj * 8 + kvh) * 256 + tid, __float_as_uint(sum)); return; }
    u32 pv[7];
    for (int spin = 0; spin < SPIN_CAP; ++spin) {
      bool ok = true;
#pragma unroll
      for (int xx = 0; xx < 7; ++xx) { pv[xx] = __float_as_uint(ld4_sc1(part + (size_t)(jj * 8 + xx) * 256 + tid)); ok = ok && pv[xx] != SENT; }
      if (__all(ok)) break;
      __builtin_amdgcn_s_sleep(1);
    }
    float tot = 0.f;
#pragma unroll
    for (int xx = 0; xx < 7; ++xx) tot += __uint_as_float(pv[xx]);
    tot += sum;
    lds[L_P + tid] = tot;
  } else {
    if (kvh != 7) return;
  }
  __syncthreads();
  if (tid < 128) {
    const float sb = bfr(lds[L_P + tid]);
    const float f = bfr(bfr(lds[L_P + 128 + tid]) + bf2f(b2in));
    const float gt = bfr(f / (1.f + __expf(-f)));
    out[(tid >> 4) * DM + jj * 16 + (tid & 15)] = f2bf(bf2f(xin) + bfr(sb * gt));
  }
}

extern "C" void kernel_launch(void* const* d_in, const int* in_sizes, int n_in,
                              void* d_out, int out_size, void* d_ws, size_t ws_size,
                              hipStream_t stream) {
  const u16* x = (const u16*)d_in[0];
  const u16* kc = (const u16*)d_in[1];
  const u16* vc = (const u16*)d_in[2];
  const u16* wq = (const u16*)d_in[3];
  const u16* wo = (const u16*)d_in[4];
  const u16* w1 = (const u16*)d_in[6];
  const u16* b1 = (const u16*)d_in[7];
  const u16* w2 = (const u16*)d_in[8];
  const u16* b2 = (const u16*)d_in[9];
  unsigned char* ws = (unsigned char*)d_ws;
  k_prep<<<256, 512, 0, stream>>>(x, wq, w1, b1, ws);
  k_main<<<256, 512, 0, stream>>>(x, kc, vc, wo, w2, b2, ws, (u16*)d_out);
}
```
